```python
import torch

def _softmax(x, axis=-1):
    return torch.softmax(x, dim=axis)
def _gelu(x):
    return torch.nn.functional.gelu(x, approximate="tanh")
def _silu(x):
    return torch.nn.functional.silu(x)
def _sigmoid(x):
    return torch.sigmoid(x)
def _layernorm(x, axis=-1, eps=1e-5):
    m = x.mean(dim=axis, keepdim=True)
    v = x.var(dim=axis, keepdim=True, unbiased=False)
    return (x - m) / torch.sqrt(v + eps)
def _scatter_add(tgt, idx, src, axis=0):
    return tgt.clone().index_add_(axis, idx, src)
def _segment_sum(src, seg, n):
    out = torch.zeros((n,) + src.shape[1:], dtype=src.dtype)
    return out.index_add_(0, seg, src)
def _pad(x, pw):
    flat = tuple(v for pair in reversed(pw) for v in pair)
    return torch.nn.functional.pad(x, flat)

kernel_name = 'sequential_preLN__gqa_block_cached__geglu_ffn__decode'

def setup_inputs():

    _w = (lambda a: (lambda t: t.to(torch.bfloat16) if t.is_floating_point() else t)(torch.as_tensor(a)))
    def _A_setup():
        B, Sq, D = 4, 1, 640
        Hq, Hkv, Sk, Dh = 32, 32, 4096, 128
        assert Hq % Hkv == 0, (Hq, Hkv)
        return {
            "x": _w((lambda *s: torch.randn(*s, dtype=torch.bfloat16) * 0.05)(B, Sq, D)),
            "k_cache": _w((lambda *s: torch.randn(*s, dtype=torch.bfloat16) * 3.2)(B, Hkv, Sk, Dh)),
            "v_cache": _w((lambda *s: torch.randn(*s, dtype=torch.bfloat16) * 0.4)(B, Hkv, Sk, Dh)),
            "wq": _w((lambda *s: torch.randn(*s, dtype=torch.bfloat16) * 3.2)(D, Hq * Dh) * (D ** -0.5)),
            "wo": _w((lambda *s: torch.randn(*s, dtype=torch.bfloat16) * 0.4)(Hq * Dh, D) * ((Hq * Dh) ** -0.5)),
        }
    _A_in = _A_setup()

    def _B_setup():
        B, S, D, F = 4, 1, 640, 1728
        return {
            "x": _w((lambda *s: torch.randn(*s, dtype=torch.bfloat16) * 0.05)(B, S, D)),
            "w_gate": _w((lambda *s: torch.randn(*s, dtype=torch.bfloat16) * 0.05)(D, F)),
            "w_up": _w((lambda *s: torch.randn(*s, dtype=torch.bfloat16) * 0.05)(D, F)),
            "w_down": _w((lambda *s: torch.randn(*s, dtype=torch.bfloat16) * 0.05)(F, D)),
        }
    _B_in = _B_setup()

    return {
        "A_x": _A_in["x"],
        "A_k_cache": _A_in["k_cache"],
        "A_v_cache": _A_in["v_cache"],
        "A_wq": _A_in["wq"],
        "A_wo": _A_in["wo"],
        "B_x": _B_in["x"],
        "B_w_gate": _B_in["w_gate"],
        "B_w_up": _B_in["w_up"],
        "B_w_down": _B_in["w_down"],
    }

def reference(A_x, A_k_cache, A_v_cache, A_wq, A_wo, B_x, B_w_gate, B_w_up, B_w_down):
    def _A_ref(x, k_cache, v_cache, wq, wo):
        B, Sq, D = x.shape
        Hq, Dh = 32, 128
        Hkv = k_cache.shape[1]
        rep = Hq // Hkv
        q = (lambda x, *d: x.permute(*d))((x @ wq).reshape(B, Sq, Hq, Dh), 0, 2, 1, 3)
        k = (lambda x, n, axis: torch.repeat_interleave(x, n, dim=axis))(k_cache, rep, axis=1)
        v = (lambda x, n, axis: torch.repeat_interleave(x, n, dim=axis))(v_cache, rep, axis=1)
        scores = torch.einsum("bhqd,bhkd->bhqk", q, k) * (Dh ** -0.5)
        p = _softmax(scores, axis=-1)
        o = torch.einsum("bhqk,bhkd->bhqd", p, v)
        return (lambda x, *d: x.permute(*d))(o, 0, 2, 1, 3).reshape(B, Sq, Hq * Dh) @ wo

    def _B_ref(x, w_gate, w_up, w_down):
        return (_gelu(x @ w_gate) * (x @ w_up)) @ w_down

    x = A_x
    h = x + _A_ref(_layernorm(x, axis=-1), A_k_cache, A_v_cache, A_wq, A_wo)
    h2 = h + _B_ref(_layernorm(h, axis=-1), B_w_gate, B_w_up, B_w_down)
    return h2
```

```cpp
#include <hip/hip_runtime.h>
#include <hip/hip_bf16.h>
#include <stdint.h>

#define MIMIC 0

typedef unsigned u32x4 __attribute__((ext_vector_type(4)));
typedef float f32x4 __attribute__((ext_vector_type(4)));
typedef __bf16 bf16x8 __attribute__((ext_vector_type(8)));
typedef __bf16 bf16x2 __attribute__((ext_vector_type(2)));
typedef unsigned short u16;

constexpr int B = 4, D = 640, H = 32, SK = 4096, DH = 128, F = 1728, HD = H * DH;
constexpr float EPS = 1e-5f;

__device__ __forceinline__ float bf2f(unsigned v) { return __builtin_bit_cast(float, v << 16); }
__device__ __forceinline__ unsigned cvtpk(float lo, float hi) {
  bf16x2 r = {(__bf16)lo, (__bf16)hi};
  return __builtin_bit_cast(unsigned, r);
}
__device__ __forceinline__ float rbf(float x) { return (float)(__bf16)x; }
template <int CTRL> __device__ __forceinline__ float dppf(float x) {
  return __builtin_bit_cast(float, __builtin_amdgcn_mov_dpp(__builtin_bit_cast(int, x), CTRL, 0xf, 0xf, true));
}
template <int N> __device__ __forceinline__ unsigned bcast(unsigned x) {
  return __builtin_amdgcn_mov_dpp(x, 0x150 + N, 0xf, 0xf, true);
}
__device__ __forceinline__ float row_sum(float x) {
  x += dppf<0x128>(x); x += dppf<0x124>(x); x += dppf<0x122>(x); x += dppf<0x121>(x); return x;
}
__device__ __forceinline__ float row_max(float x) {
  x = fmaxf(x, dppf<0x128>(x)); x = fmaxf(x, dppf<0x124>(x)); x = fmaxf(x, dppf<0x122>(x)); x = fmaxf(x, dppf<0x121>(x)); return x;
}
__device__ __forceinline__ float xrow_sum(float x) {
  auto s = __builtin_amdgcn_permlane16_swap(__float_as_uint(x), __float_as_uint(x), false, false);
  x = __uint_as_float(s[0]) + __uint_as_float(s[1]);
  auto t = __builtin_amdgcn_permlane32_swap(__float_as_uint(x), __float_as_uint(x), false, false);
  return __uint_as_float(t[0]) + __uint_as_float(t[1]);
}
__device__ __forceinline__ float xrow_max(float x) {
  auto s = __builtin_amdgcn_permlane16_swap(__float_as_uint(x), __float_as_uint(x), false, false);
  x = fmaxf(__uint_as_float(s[0]), __uint_as_float(s[1]));
  auto t = __builtin_amdgcn_permlane32_swap(__float_as_uint(x), __float_as_uint(x), false, false);
  return fmaxf(__uint_as_float(t[0]), __uint_as_float(t[1]));
}
__device__ __forceinline__ float wave_sum(float x) { return xrow_sum(row_sum(x)); }
__device__ __forceinline__ float wave_max(float x) { return xrow_max(row_max(x)); }
__device__ __forceinline__ float dot2(unsigned a, unsigned b, float c) {
  return __builtin_amdgcn_fdot2_f32_bf16(__builtin_bit_cast(bf16x2, a), __builtin_bit_cast(bf16x2, b), c, false);
}
__device__ __forceinline__ f32x4 mfma(u32x4 a, u32x4 b, f32x4 c) {
  return __builtin_amdgcn_mfma_f32_16x16x32_bf16(__builtin_bit_cast(bf16x8, a), __builtin_bit_cast(bf16x8, b), c, 0, 0, 0);
}
__device__ __forceinline__ u32x4 ldnt(const void* p) { return __builtin_nontemporal_load((const u32x4*)p); }
__device__ __forceinline__ u32x4 ld(const void* p) { return *(const u32x4*)p; }

template <int I> __device__ __forceinline__ u32x4 selfrag(unsigned vdup, const u32x4& m) {
  unsigned t = bcast<I>(vdup);
  return u32x4{t & m[0], t & m[1], t & m[2], t & m[3]};
}

template <int I, int N, class Fn> __device__ __forceinline__ void static_for(Fn&& f) {
  if constexpr (I < N) { f(std::integral_constant<int, I>{}); static_for<I + 1, N>(f); }
}

constexpr int WIN = 16;

__global__ __launch_bounds__(512, 2) void attn_kernel(const u16* __restrict__ x, const u16* __restrict__ kc, const u16* __restrict__ vc,
                                                      const u16* __restrict__ wq, float* __restrict__ ws) {
  __shared__ __attribute__((aligned(16))) float lds[8 * 128 + 64];
  __shared__ __attribute__((aligned(16))) u16 qs[128];
  const int bid = blockIdx.x;
  const int xcd = bid & 7, idx = bid >> 3;
  const int h = xcd * 4 + (idx >> 3), b = (idx >> 1) & 3, half = idx & 1;
  const int tid = threadIdx.x, lane = tid & 63, w = __builtin_amdgcn_readfirstlane(tid >> 6);
  const int g = lane >> 4, c = lane & 15;

  const unsigned* x32 = (const unsigned*)(x + b * D);
  unsigned xs[5];
#pragma unroll
  for (int t = 0; t < 5; ++t) xs[t] = x32[lane + 64 * t];
  unsigned xa = x[b * D + 80 * w + 4 * c + g];
  unsigned xb = x[b * D + 80 * w + 64 + 4 * (c & 3) + g];
  u32x4 wqf[20];
  const u16* wqp = wq + (size_t)(80 * w + g) * HD + h * DH + 8 * c;
#pragma unroll
  for (int i = 0; i < 20; ++i) wqf[i] = ld(wqp + (size_t)(4 * i) * HD);
  const size_t kvoff = ((size_t)(b * H + h) * SK + half * 2048 + w * 256 + g) * DH + 8 * c;
  const u16* kp = kc + kvoff;
  const u16* vp = vc + kvoff;
  u32x4 buf[WIN];
#pragma unroll
  for (int i = 0; i < WIN; ++i) buf[i] = ldnt(kp + (size_t)(4 * i) * DH);
  __builtin_amdgcn_sched_barrier(0);

  float s = 0.f;
#pragma unroll
  for (int t = 0; t < 5; ++t) s += bf2f(xs[t] & 0xffff) + bf2f(xs[t] >> 16);
  float mean = wave_sum(s) * (1.f / D);
  float v = 0.f;
#pragma unroll
  for (int t = 0; t < 5; ++t) { float a = bf2f(xs[t] & 0xffff) - mean, bb = bf2f(xs[t] >> 16) - mean; v += a * a + bb * bb; }
  float var = wave_sum(v) * (1.f / D);
  float xan, xbn;
  if (MIMIC) {
    float mb = rbf(mean), sd = rbf(sqrtf(rbf(rbf(var) + EPS)));
    xan = rbf(bf2f(xa) - mb) / sd; xbn = rbf(bf2f(xb) - mb) / sd;
  } else {
    float rs = rsqrtf(var + EPS);
    xan = (bf2f(xa) - mean) * rs; xbn = (bf2f(xb) - mean) * rs;
  }
  unsigned xad = cvtpk(xan, xan), xbd = cvtpk(xbn, xbn);
  u32x4 msk;
#pragma unroll
  for (int r = 0; r < 4; ++r) msk[r] = (c < 8 && (c >> 1) == r) ? ((c & 1) ? 0xffff0000u : 0x0000ffffu) : 0u;

  {
    f32x4 acc0 = {0, 0, 0, 0}, acc1 = {0, 0, 0, 0};
    static_for<0, 20>([&](auto I) {
      constexpr int i = I.value;
      u32x4 a = selfrag<i & 15>(i < 16 ? xad : xbd, msk);
      if (i & 1) acc1 = mfma(a, wqf[i], acc1); else acc0 = mfma(a, wqf[i], acc0);
    });
    acc0 += acc1;
    if (lane < 32) *(f32x4*)&lds[w * 128 + 8 * c + 4 * g] = acc0;
  }
  __syncthreads();
  if (tid < 128) {
    float q = 0.f;
#pragma unroll
    for (int i = 0; i < 8; ++i) q += lds[i * 128 + tid];
    qs[tid] = (u16)(cvtpk(q, q) & 0xffff);
  }
  __syncthreads();
  const u32x4 q = *(const u32x4*)&qs[8 * c];

  float sc[4];
#pragma unroll 1
  for (int it = 0; it < 4; ++it) {
    const u16* np = (it < 3) ? kp + (size_t)(64 * (it + 1)) * DH : vp;
    float keep = 0.f;
    static_for<0, 16>([&](auto I) {
      constexpr int i = I.value;
      u32x4 k = buf[i];
      float d = dot2(k[0], q[0], 0.f);
      d = dot2(k[1], q[1], d); d = dot2(k[2], q[2], d); d = dot2(k[3], q[3], d);
      buf[i] = ldnt(np + (size_t)(4 * i) * DH);
      d = row_sum(d);
      keep = (c == i) ? d : keep;
    });
    if (it == 0) sc[0] = keep; else if (it == 1) sc[1] = keep; else if (it == 2) sc[2] = keep; else sc[3] = keep;
  }
  constexpr float SCALE = 0.08838834764831845f, LOG2E = 1.4426950408889634f;
#pragma unroll
  for (int i = 0; i < 4; ++i) sc[i] = MIMIC ? rbf(rbf(sc[i]) * SCALE) * LOG2E : sc[i] * (SCALE * LOG2E);
  float m = wave_max(fmaxf(fmaxf(sc[0], sc[1]), fmaxf(sc[2], sc[3])));
  unsigned pd[4];
  float l = 0.f;
#pragma unroll
  for (int i = 0; i < 4; ++i) { float p = __builtin_amdgcn_exp2f(sc[i] - m); pd[i] = cvtpk(p, p); l += bf2f(pd[i] & 0xffff); }
  l = wave_sum(l);
  f32x4 acc0 = {0, 0, 0, 0}, acc1 = {0, 0, 0, 0};
#pragma unroll 1
  for (int it = 0; it < 4; ++it) {
    const u16* np = vp + (size_t)(64 * (it + 1)) * DH;
    unsigned pcur = it == 0 ? pd[0] : it == 1 ? pd[1] : it == 2 ? pd[2] : pd[3];
    if (it < 3) {
      static_for<0, 16>([&](auto I) {
        constexpr int i = I.value;
        u32x4 a = selfrag<i>(pcur, msk);
        if (i & 1) acc1 = mfma(a, buf[i], acc1); else acc0 = mfma(a, buf[i], acc0);
        buf[i] = ldnt(np + (size_t)(4 * i) * DH);
      });
    } else {
      static_for<0, 16>([&](auto I) {
        constexpr int i = I.value;
        u32x4 a = selfrag<i>(pcur, msk);
        if (i & 1) acc1 = mfma(a, buf[i], acc1); else acc0 = mfma(a, buf[i], acc0);
      });
    }
  }
  acc0 += acc1;
  __syncthreads();
  if (lane < 32) *(f32x4*)&lds[w * 128 + 8 * c + 4 * g] = acc0;
  if (lane == 0) { lds[1024 + 2 * w] = m; lds[1024 + 2 * w + 1] = l; }
  __syncthreads();
  if (tid < 128) {
    float M = -INFINITY;
#pragma unroll
    for (int i = 0; i < 8; ++i) M = fmaxf(M, lds[1024 + 2 * i]);
    float L = 0.f, o = 0.f;
#pragma unroll
    for (int i = 0; i < 8; ++i) { float wt = __builtin_amdgcn_exp2f(lds[1024 + 2 * i] - M); L += wt * lds[1024 + 2 * i + 1]; o += wt * lds[i * 128 + tid]; }
    const int slot = (b * H + h) * 2 + half;
    ws[slot * 128 + tid] = o;
    if (tid == 0) { ws[256 * 128 + slot * 2] = M; ws[256 * 128 + slot * 2 + 1] = L; }
  }
}

__global__ __launch_bounds__(320) void wo_kernel(const float* __restrict__ ws, const u16* __restrict__ wo, float* __restrict__ yp) {
  __shared__ float o[4][128];
  const int h = blockIdx.x, tid = threadIdx.x;
  for (int i = tid; i < 512; i += 320) {
    int b = i >> 7, d = i & 127;
    int s0 = (b * H + h) * 2;
    float m0 = ws[256 * 128 + s0 * 2], l0 = ws[256 * 128 + s0 * 2 + 1], m1 = ws[256 * 128 + s0 * 2 + 2], l1 = ws[256 * 128 + s0 * 2 + 3];
    float M = fmaxf(m0, m1), w0 = exp2f(m0 - M), w1 = exp2f(m1 - M);
    float val = (w0 * ws[s0 * 128 + d] + w1 * ws[(s0 + 1) * 128 + d]) / (w0 * l0 + w1 * l1);
    o[b][d] = MIMIC ? rbf(val) : val;
  }
  __syncthreads();
  float acc[4][2] = {};
  const unsigned* wp = (const unsigned*)(wo + (size_t)h * DH * D) + tid;
#pragma unroll 16
  for (int k = 0; k < 128; ++k) {
    unsigned wv = wp[k * (D / 2)];
    float w0 = bf2f(wv & 0xffff), w1 = bf2f(wv >> 16);
#pragma unroll
    for (int b = 0; b < 4; ++b) { acc[b][0] += o[b][k] * w0; acc[b][1] += o[b][k] * w1; }
  }
#pragma unroll
  for (int b = 0; b < 4; ++b) { yp[(h * 4 + b) * D + 2 * tid] = acc[b][0]; yp[(h * 4 + b) * D + 2 * tid + 1] = acc[b][1]; }
}

__device__ __forceinline__ float gelu_tanh(float x) {
  float u = 0.7978845608028654f * (x + 0.044715f * x * x * x);
  float t = 1.f - 2.f / (1.f + __expf(2.f * u));
  return 0.5f * x * (1.f + t);
}

__global__ __launch_bounds__(256) void gu_kernel(const u16* __restrict__ x, const float* __restrict__ yp, const u16* __restrict__ wg, const u16* __restrict__ wu,
                                                 float* __restrict__ hout, float* __restrict__ gout) {
  __shared__ float hs[4][D];
  __shared__ float red[8][4][32][2];
  const int tid = threadIdx.x, lane = tid & 63, w = tid >> 6;
  for (int i = tid; i < 4 * D; i += 256) {
    int b = i / D, n = i % D;
    float s = 0.f;
    for (int h = 0; h < 32; ++h) s += yp[(h * 4 + b) * D + n];
    float hv = MIMIC ? rbf(bf2f(x[i]) + rbf(s)) : bf2f(x[i]) + s;
    hs[b][n] = hv;
    if (blockIdx.x == 0) hout[i] = hv;
  }
  __syncthreads();
  {
    int b = w;
    float s = 0.f;
    for (int i = lane; i < D; i += 64) s += hs[b][i];
    float mean = wave_sum(s) * (1.f / D);
    float v = 0.f;
    for (int i = lane; i < D; i += 64) { float a = hs[b][i] - mean; v += a * a; }
    float var = wave_sum(v) * (1.f / D);
    if (MIMIC) {
      float mb = rbf(mean), sd = rbf(sqrtf(rbf(rbf(var) + EPS)));
      for (int i = lane; i < D; i += 64) hs[b][i] = rbf(rbf(hs[b][i] - mb) / sd);
    } else {
      float rs = rsqrtf(var + EPS);
      for (int i = lane; i < D; i += 64) hs[b][i] = (hs[b][i] - mean) * rs;
    }
  }
  __syncthreads();
  const int col = tid & 31, ks = tid >> 5;
  const int n = blockIdx.x * 32 + col;
  float ag[4] = {}, au[4] = {};
  for (int k = ks * 80; k < ks * 80 + 80; ++k) {
    float a = bf2f(wg[(size_t)k * F + n]), u = bf2f(wu[(size_t)k * F + n]);
#pragma unroll
    for (int b = 0; b < 4; ++b) { ag[b] += hs[b][k] * a; au[b] += hs[b][k] * u; }
  }
#pragma unroll
  for (int b = 0; b < 4; ++b) { red[ks][b][col][0] = ag[b]; red[ks][b][col][1] = au[b]; }
  __syncthreads();
  if (tid < 128) {
    int b = tid >> 5;
    float a = 0.f, u = 0.f;
    for (int i = 0; i < 8; ++i) { a += red[i][b][col][0]; u += red[i][b][col][1]; }
    float gv = MIMIC ? rbf(rbf(gelu_tanh(rbf(a))) * rbf(u)) : gelu_tanh(a) * u;
    gout[b * F + n] = gv;
  }
}

__global__ __launch_bounds__(256) void down_kernel(const float* __restrict__ hin, const float* __restrict__ gin, const u16* __restrict__ wd, u16* __restrict__ out) {
  __shared__ float gs[4][F];
  __shared__ float red[16][4][16];
  const int tid = threadIdx.x;
  for (int i = tid; i < 4 * F; i += 256) gs[i / F][i % F] = gin[i];
  __syncthreads();
  const int col = tid & 15, ks = tid >> 4;
  const int n = blockIdx.x * 16 + col;
  float a[4] = {};
  for (int k = ks * 108; k < ks * 108 + 108; ++k) {
    float wv = bf2f(wd[(size_t)k * D + n]);
#pragma unroll
    for (int b = 0; b < 4; ++b) a[b] += gs[b][k] * wv;
  }
#pragma unroll
  for (int b = 0; b < 4; ++b) red[ks][b][col] = a[b];
  __syncthreads();
  if (tid < 64) {
    int b = tid >> 4;
    float s = 0.f;
    for (int i = 0; i < 16; ++i) s += red[i][b][col];
    float r = MIMIC ? hin[b * D + n] + rbf(s) : hin[b * D + n] + s;
    out[b * D + n] = (u16)(cvtpk(r, r) & 0xffff);
  }
}

extern "C" void kernel_launch(void* const* d_in, const int* in_sizes, int n_in, void* d_out, int out_size, void* d_ws, size_t ws_size,
                              hipStream_t stream) {
  const u16* x = (const u16*)d_in[0];
  const u16* kc = (const u16*)d_in[1];
  const u16* vc = (const u16*)d_in[2];
  const u16* wq = (const u16*)d_in[3];
  const u16* wo = (const u16*)d_in[4];
  const u16* wg = (const u16*)d_in[6];
  const u16* wu = (const u16*)d_in[7];
  const u16* wd = (const u16*)d_in[8];
  float* ws = (float*)d_ws;
  float* yp = ws + 65536;
  float* hb = yp + 32 * 4 * D;
  float* gb = hb + 4 * D;
  attn_kernel<<<256, 512, 0, stream>>>(x, kc, vc, wq, ws);
  wo_kernel<<<32, 320, 0, stream>>>(ws, wo, yp);
  gu_kernel<<<54, 256, 0, stream>>>(x, yp, wg, wu, hb, gb);
  down_kernel<<<40, 256, 0, stream>>>(hb, gb, wd, (u16*)d_out);
}
```

```cpp
#include <hip/hip_runtime.h>
#include <stdint.h>
#include <type_traits>

#define STAMPS 0
#define DSTEADY 6
#define DTAIL 10
#define TAUX 2
#define WAUX 0

typedef unsigned u32x4 __attribute__((ext_vector_type(4)));
typedef unsigned u32x2 __attribute__((ext_vector_type(2)));
typedef float f32x4 __attribute__((ext_vector_type(4)));
typedef float f32x2 __attribute__((ext_vector_type(2)));
typedef __bf16 bf16x8 __attribute__((ext_vector_type(8)));
typedef __bf16 bf16x2 __attribute__((ext_vector_type(2)));
typedef short s16x4 __attribute__((ext_vector_type(4)));
typedef unsigned short u16;
typedef unsigned long long u64;
#define LAS __attribute__((address_space(3)))

constexpr int D = 640, H = 32, SK = 4096, DH = 128, F = 1728, HD = H * DH;
constexpr float EPS = 1e-5f;

constexpr int WS_EPOCH = 0;
constexpr int WS_ML = 4096;
constexpr int WS_H = 32768;
constexpr int WS_G = 65536;
constexpr int WS_O = 131072;
constexpr int WS_Y = WS_O + 32 * 8192;
constexpr int WS_Q = WS_Y + 160 * 4096;
constexpr int WS_FLAG = 1024;
constexpr int WS_BYTES = WS_Q + 128 * 512;
constexpr int WS_STAMP = 4 << 20;

constexpr int L_Q = 0;
constexpr int L_OP = 256;
constexpr int L_RED = 2304;
constexpr int L_ACT = L_RED + 8192;
constexpr int OROW = 256 + 16, HROW = 1280 + 32, GROW = 3584 + 32;
constexpr int L_WT = L_ACT + 4 * GROW + 64;
constexpr int L_BYTES = L_WT + 81920;
static_assert(L_WT % 16 == 0 && L_ACT % 16 == 0, "align");
#define PPRE 32
#define CUR 6

__device__ __forceinline__ float bf2f(unsigned v) { return __builtin_bit_cast(float, v << 16); }
__device__ __forceinline__ float bfhi(unsigned v) { return __builtin_bit_cast(float, v & 0xffff0000u); }
__device__ __forceinline__ unsigned cvtpk(float lo, float hi) {
  bf16x2 r = {(__bf16)lo, (__bf16)hi};
  return __builtin_bit_cast(unsigned, r);
}
__device__ __forceinline__ float rbf(float x) { return (float)(__bf16)x; }
template <int CTRL> __device__ __forceinline__ float dppf(float x) {
  return __builtin_bit_cast(float, __builtin_amdgcn_mov_dpp(__builtin_bit_cast(int, x), CTRL, 0xf, 0xf, true));
}
template <int N> __device__ __forceinline__ unsigned bcast(unsigned x) { return __builtin_amdgcn_mov_dpp(x, 0x150 + N, 0xf, 0xf, true); }
__device__ __forceinline__ float row_sum(float x) {
  x += dppf<0x128>(x); x += dppf<0x124>(x); x += dppf<0x122>(x); x += dppf<0x121>(x); return x;
}
__device__ __forceinline__ float row_max(float x) {
  x = fmaxf(x, dppf<0x128>(x)); x = fmaxf(x, dppf<0x124>(x)); x = fmaxf(x, dppf<0x122>(x)); x = fmaxf(x, dppf<0x121>(x)); return x;
}
__device__ __forceinline__ float x16_sum(float x) {
  auto s = __builtin_amdgcn_permlane16_swap(__float_as_uint(x), __float_as_uint(x), false, false);
  return __uint_as_float(s[0]) + __uint_as_float(s[1]);
}
__device__ __forceinline__ float x32_sum(float x) {
  auto t = __builtin_amdgcn_permlane32_swap(__float_as_uint(x), __float_as_uint(x), false, false);
  return __uint_as_float(t[0]) + __uint_as_float(t[1]);
}
__device__ __forceinline__ float xrow_max(float x) {
  auto s = __builtin_amdgcn_permlane16_swap(__float_as_uint(x), __float_as_uint(x), false, false);
  x = fmaxf(__uint_as_float(s[0]), __uint_as_float(s[1]));
  auto t = __builtin_amdgcn_permlane32_swap(__float_as_uint(x), __float_as_uint(x), false, false);
  return fmaxf(__uint_as_float(t[0]), __uint_as_float(t[1]));
}
__device__ __forceinline__ float wave_sum(float x) { return x32_sum(x16_sum(row_sum(x))); }
__device__ __forceinline__ float wave_max(float x) { return xrow_max(row_max(x)); }
__device__ __forceinline__ float dot2(unsigned a, unsigned b, float c) {
  return __builtin_amdgcn_fdot2_f32_bf16(__builtin_bit_cast(bf16x2, a), __builtin_bit_cast(bf16x2, b), c, false);
}
__device__ __forceinline__ f32x4 mfma(u32x4 a, u32x4 b, f32x4 c) {
  return __builtin_amdgcn_mfma_f32_16x16x32_bf16(__builtin_bit_cast(bf16x8, a), __builtin_bit_cast(bf16x8, b), c, 0, 0, 0);
}
template <int I> __device__ __forceinline__ u32x4 selfrag(unsigned vdup, const u32x4& m) {
  unsigned t = bcast<I>(vdup);
  return u32x4{t & m[0], t & m[1], t & m[2], t & m[3]};
}
template <int I, int N, class Fn> __device__ __forceinline__ void static_for(Fn&& f) {
  if constexpr (I < N) { f(std::integral_constant<int, I>{}); static_for<I + 1, N>(f); }
}
typedef __amdgpu_buffer_rsrc_t rsrc_t;
__device__ __forceinline__ rsrc_t mkrs(const void* p, unsigned bytes) { return __builtin_amdgcn_make_buffer_rsrc((void*)p, 0, bytes, 0x00020000); }
template <int AUX> __device__ __forceinline__ u32x4 bld4(rsrc_t r, int voff, int soff) { return __builtin_amdgcn_raw_buffer_load_b128(r, voff, soff, AUX); }
template <int AUX> __device__ __forceinline__ u32x2 bld2(rsrc_t r, int voff, int soff) { return __builtin_amdgcn_raw_buffer_load_b64(r, voff, soff, AUX); }
constexpr int SC1 = 16, NT = 2;
__device__ __forceinline__ void wg_barrier() {
  asm volatile("s_waitcnt lgkmcnt(0)" ::: "memory");
  __builtin_amdgcn_s_barrier();
  asm volatile("" ::: "memory");
}
constexpr unsigned SPIN_CAP = 1u << 20;

__device__ __forceinline__ float gelu_tanh(float x) {
  float u = 0.7978845608028654f * (x + 0.044715f * x * x * x);
  float e = __builtin_amdgcn_exp2f(u * 2.885390081777927f);
  float t = 1.f - 2.f * __builtin_amdgcn_rcpf(1.f + e);
  return 0.5f * x * (1.f + t);
}

#define STAMP(k) do {} while (0)
#define STAMP_DECL do {} while (0)
#define STAMP_FLUSH do {} while (0)

__device__ __forceinline__ u32x4 trfrag(const unsigned char* p, int rowstride4) {
  s16x4 lo = __builtin_amdgcn_ds_read_tr16_b64_v4i16((LAS s16x4*)p);
  s16x4 hi = __builtin_amdgcn_ds_read_tr16_b64_v4i16((LAS s16x4*)(p + rowstride4));
  u32x2 l2 = __builtin_bit_cast(u32x2, lo), h2 = __builtin_bit_cast(u32x2, hi);
  return u32x4{l2[0], l2[1], h2[0], h2[1]};
}
__device__ __forceinline__ void st_gran2(rsrc_t rws, int off, float a, float b, unsigned tag) {
  __builtin_amdgcn_raw_buffer_store_b128(u32x4{__float_as_uint(a), tag, __float_as_uint(b), tag}, rws, off, 0, SC1);
}
constexpr int k_issued(int i) {
  int a = PPRE + DSTEADY + i / CUR, b = i + DSTEADY + 1;
  return a > b ? a : b;
}

__global__ __launch_bounds__(512, 2) void fused_kernel(const u16* __restrict__ x, const u16* __restrict__ kc, const u16* __restrict__ vc,
                                                       const u16* __restrict__ wq, const u16* __restrict__ wo, const u16* __restrict__ wg,
                                                       const u16* __restrict__ wu, const u16* __restrict__ wd, u16* __restrict__ out,
                                                       unsigned char* ws) {
  __shared__ __attribute__((aligned(16))) unsigned char lds[L_BYTES];
  const int bid = blockIdx.x;
  const int xcd = bid & 7, slot = bid >> 3;
  const int h = xcd * 4 + (slot >> 3), b = (slot >> 1) & 3, half = slot & 1, jj = slot & 7;
  const int tid = threadIdx.x, lane = tid & 63, wave = __builtin_amdgcn_readfirstlane(tid >> 6);
  const int g = lane >> 4, c = lane & 15;
  STAMP_DECL;
  STAMP(0);
  int role = 0, ridx = 0;
  {
    const int i2 = slot >> 1;
    if (!half) { role = 3; ridx = i2 * 8 + xcd; }
    else if (i2 < 8) { int pr = (i2 >> 1) * 8 + xcd; if (pr < 27) { role = 1; ridx = pr * 2 + (i2 & 1); } }
    else { int grp = ((i2 - 8) >> 2) * 8 + xcd; if (grp < 10) { role = 2; ridx = grp * 4 + (i2 & 3); } }
  }
  const rsrc_t rws = mkrs(ws, WS_BYTES);
  unsigned tag = __builtin_amdgcn_raw_buffer_load_b32(rws, WS_EPOCH, 0, SC1);

  if (wave < 4) {
    u32x4 msk;
#pragma unroll
    for (int r = 0; r < 4; ++r) msk[r] = (c < 8 && (c >> 1) == r) ? ((c & 1) ? 0xffff0000u : 0x0000ffffu) : 0u;
    const size_t kvoff = ((size_t)(b * H + h) * SK + half * 2048) * DH;
    const rsrc_t rk = mkrs(kc + kvoff, 512 * 1024), rv = mkrs(vc + kvoff, 512 * 1024);
    const int voff = wave * 1024 + lane * 16;
    constexpr int DS = DSTEADY, DT = DTAIL;
    u32x4 k0[64 + DS];
    static_for<0, DS>([&](auto J) { k0[J.value] = bld4<NT>(rk, voff, J.value * 4096); });
    __builtin_amdgcn_sched_barrier(0);
    static_for<0, PPRE>([&](auto I) {
      constexpr int i = I.value;
      asm volatile("" : "+v"(k0[i]));
      __builtin_amdgcn_sched_barrier(0);
      k0[i + DS] = bld4<NT>(rk, voff, (i + DS) * 4096);
      __builtin_amdgcn_sched_barrier(0);
    });
    STAMP(10);
    wg_barrier();
    const u32x4 q = *(const u32x4*)(lds + L_Q + 16 * c);
    asm volatile("v_add_u32 %0, 1, %0" : "+v"(tag));
    STAMP(1);
    float sc[8];
    auto kstep = [&](u32x4 k) -> float {
      float d = dot2(k[0], q[0], 0.f);
      d = dot2(k[1], q[1], d); d = dot2(k[2], q[2], d); d = dot2(k[3], q[3], d);
      return row_sum(d);
    };
    {
      float kk[4] = {0.f, 0.f, 0.f, 0.f};
      static_for<0, 64>([&](auto I) {
        constexpr int i = I.value;
        constexpr int lo = i == 0 ? PPRE + DS : k_issued(i - 1), hi = k_issued(i);
        if constexpr (hi > lo) {
          asm volatile("" : "+v"(kk[0]), "+v"(kk[1]), "+v"(kk[2]), "+v"(kk[3]));
          static_for<lo, hi>([&](auto J) {
            constexpr int j = J.value;
            asm volatile("" : "+v"(k0[j - DS]));
            __builtin_amdgcn_sched_barrier(0);
            k0[j] = bld4<NT>(rk, voff, j * 4096);
            __builtin_amdgcn_sched_barrier(0);
          });
        }
        float d = kstep(k0[i]);
        kk[i >> 4] = (c == (i & 15)) ? d : kk[i >> 4];
      });
      static_assert(k_issued(63) == 64 + DS, "catch-up must finish inside the peeled part");
      sc[4] = kk[0]; sc[5] = kk[1]; sc[6] = kk[2]; sc[7] = kk[3];
    }
    u32x4 ring[32];
#pragma unroll
    for (int j = 0; j < DS; ++j) ring[j] = k0[64 + j];
#pragma unroll 1
    for (int it = 2; it < 4; ++it) {
#pragma unroll
      for (int i = 0; i < 6; ++i) sc[i] = sc[i + 2];
      const rsrc_t rn = it == 3 ? rv : rk;
      const int so = it * 32 * 4096, son = it == 3 ? 0 : so + 32 * 4096;
      float ka = 0.f, kb = 0.f;
      static_for<0, 32>([&](auto I) {
        constexpr int i = I.value;
        u32x4 cur = ring[i];
        asm volatile("" : "+v"(cur));
        __builtin_amdgcn_sched_barrier(0);
        if constexpr (i + DS < 32) ring[i + DS] = bld4<NT>(rk, voff, so + (i + DS) * 4096);
        else ring[i + DS - 32] = bld4<NT>(rn, voff, son + (i + DS - 32) * 4096);
        float d = kstep(cur);
        if constexpr (i < 16) { ka = (c == i) ? d : ka; asm volatile("" : "+v"(ka)); } else { kb = (c == i - 16) ? d : kb; asm volatile("" : "+v"(kb)); }
        __builtin_amdgcn_sched_barrier(0);
      });
      sc[6] = ka; sc[7] = kb;
    }
    STAMP(2);
    constexpr float SCALE = 0.08838834764831845f, LOG2E = 1.4426950408889634f;
    float m = -INFINITY;
#pragma unroll
    for (int i = 0; i < 8; ++i) { sc[i] = rbf(rbf(sc[i]) * SCALE) * LOG2E; m = fmaxf(m, sc[i]); }
    m = wave_max(m);
    unsigned pd[8];
    float l = 0.f;
#pragma unroll
    for (int i = 0; i < 8; ++i) { float p = __builtin_amdgcn_exp2f(sc[i] - m); pd[i] = cvtpk(p, p); l += bfhi(pd[i]); }
    l = wave_sum(l);
    if (lane == 0) st_gran2(rws, WS_ML + (b * H + h) * 128 + half * 64 + wave * 16, m, l, tag);
    f32x4 acc0 = {0, 0, 0, 0}, acc1 = {0, 0, 0, 0};
#pragma unroll 1
    for (int it = 0; it < 3; ++it) {
      const int so = it * 32 * 4096;
      static_for<0, 32>([&](auto I) {
        constexpr int i = I.value;
        u32x4 a = selfrag<i & 15>(pd[i >> 4], msk);
        u32x4 cur = ring[i];
        asm volatile("" : "+v"(cur));
        __builtin_amdgcn_sched_barrier(0);
        ring[(i + DS) % 32] = bld4<NT>(rv, voff, so + (i + DS) * 4096);
        if constexpr (i & 1) { acc1 = mfma(a, cur, acc1); asm volatile("" : "+v"(acc1)); } else { acc0 = mfma(a, cur, acc0); asm volatile("" : "+v"(acc0)); }
        __builtin_amdgcn_sched_barrier(0);
      });
#pragma unroll
      for (int i = 0; i < 6; ++i) pd[i] = pd[i + 2];
    }
    u32x4 wt[10];
    {
      const int so = 3 * 32 * 4096;
      const rsrc_t rW = mkrs(wo, role == 3 ? HD * D * 2 : 0);
#pragma unroll
      for (int j = DS; j < DT; ++j) ring[j] = bld4<NT>(rv, voff, so + j * 4096);
      static_for<0, 32>([&](auto I) {
        constexpr int i = I.value;
        u32x4 a = selfrag<i & 15>(pd[i >> 4], msk);
        u32x4 cur = ring[i];
        asm volatile("" : "+v"(cur));
        __builtin_amdgcn_sched_barrier(0);
        if constexpr (i + DT < 32) ring[i + DT] = bld4<NT>(rv, voff, so + (i + DT) * 4096);
        if constexpr (i + DT == 32) {
#pragma unroll
          for (int n = 0; n < 10; ++n) {
            const int p = n * 64 + lane, row = p / 20, ch = p - row * 20;
            wt[n] = bld4<TAUX>(rW, row * (D * 2) + ch * 16, (h * DH + wave * 32) * (D * 2) + b * 320);
          }
        }
        if constexpr (i & 1) { acc1 = mfma(a, cur, acc1); asm volatile("" : "+v"(acc1)); } else { acc0 = mfma(a, cur, acc0); asm volatile("" : "+v"(acc0)); }
        __builtin_amdgcn_sched_barrier(0);
      });
    }
    acc0 += acc1;
    if (lane < 32) *(f32x4*)(lds + L_OP + (wave * 128 + 8 * c + 4 * g) * 4) = acc0;
    STAMP(3);
    wg_barrier();
    if (role == 3) {
#pragma unroll
      for (int n = 0; n < 10; ++n) *(u32x4*)(lds + L_WT + wave * 10240 + n * 1024 + lane * 16) = wt[n];
    }
  } else {
    float scl[4] = {0.f, 0.f, 0.f, 0.f};
    if (wave == 4) {
      u32x4 xs[5];
#pragma unroll
      for (int t = 0; t < 5; ++t) xs[t] = *(const u32x4*)(x + g * D + c * 40 + t * 8);
      __builtin_amdgcn_sched_barrier(0);
      u32x4 wf[20];
      {
        const rsrc_t rq = mkrs(wq, D * HD * 2);
        const int vo = (lane >> 1) * HD * 2 + (lane & 1) * 16;
        const int so = (h * DH + 16 * jj) * 2;
#pragma unroll
        for (int i = 0; i < 20; ++i) wf[i] = bld4<NT>(rq, vo, so + i * 32 * HD * 2);
      }
      __builtin_amdgcn_sched_barrier(0);
      asm volatile("v_add_u32 %0, 1, %0" : "+v"(tag));
      float s = 0.f;
#pragma unroll
      for (int t = 0; t < 5; ++t)
#pragma unroll
        for (int r = 0; r < 4; ++r) s += bf2f(xs[t][r] & 0xffff) + bfhi(xs[t][r]);
      const float mean = row_sum(s) * (1.f / D);
      float v = 0.f;
#pragma unroll
      for (int t = 0; t < 5; ++t)
#pragma unroll
        for (int r = 0; r < 4; ++r) { float a = bf2f(xs[t][r] & 0xffff) - mean, bb = bfhi(xs[t][r]) - mean; v += a * a + bb * bb; }
      const float var = row_sum(v) * (1.f / D);
      const float mb = rbf(mean), rs = 1.f / rbf(sqrtf(rbf(rbf(var) + EPS)));
#pragma unroll
      for (int t = 0; t < 5; ++t) {
        u32x4 xn;
#pragma unroll
        for (int r = 0; r < 4; ++r) {
          const unsigned d = cvtpk(bf2f(xs[t][r] & 0xffff) - mb, bfhi(xs[t][r]) - mb);
          xn[r] = cvtpk(bf2f(d & 0xffff) * rs, bfhi(d) * rs);
        }
        *(u32x4*)(lds + L_ACT + g * HROW + c * 80 + t * 16) = xn;
      }
      f32x4 a0 = {0, 0, 0, 0}, a1 = {0, 0, 0, 0};
#pragma unroll
      for (int i = 0; i < 20; ++i) *(u32x4*)(lds + L_WT + i * 1024 + lane * 16) = wf[i];
#pragma unroll
      for (int i = 0; i < 20; ++i) {
        const u32x4 af = trfrag(lds + L_WT + i * 1024 + g * 256 + c * 8, 128);
        const u32x4 xb = *(const u32x4*)(lds + L_ACT + (c & 3) * HROW + i * 64 + g * 16);
        if (i & 1) a1 = mfma(af, xb, a1); else a0 = mfma(af, xb, a0);
      }
      a0 += a1;
      if (c < 4)
        __builtin_amdgcn_raw_buffer_store_b128(u32x4{cvtpk(a0[0], a0[1]), tag, cvtpk(a0[2], a0[3]), tag}, rws,
                                               WS_Q + (h * 4 + c) * 512 + jj * 64 + g * 16, 0, SC1);
      STAMP(10);
      u32x4 qv;
      for (unsigned spins = 0; spins < SPIN_CAP; ++spins) {
        qv = bld4<SC1>(rws, WS_Q + (h * 4 + b) * 512 + (lane & 31) * 16, 0);
        if (__all(qv[1] == tag && qv[3] == tag)) break;
        __builtin_amdgcn_s_sleep(4);
        asm volatile("" ::: "memory");
      }
      if (lane < 32) *(u32x2*)(lds + L_Q + lane * 8) = u32x2{qv[0], qv[2]};
      STAMP(1);
    } else {
      asm volatile("v_add_u32 %0, 1, %0" : "+v"(tag));
    }
    wg_barrier();
    if (wave == 4) {
      u32x4 gm;
      for (unsigned spins = 0; spins < SPIN_CAP; ++spins) {
        __builtin_amdgcn_s_sleep(64);
        gm = bld4<SC1>(rws, WS_ML + (b * H + h) * 128 + (lane & 7) * 16, 0);
        const bool ok = gm[1] == tag && gm[3] == tag;
        if (__all(ok)) break;
        asm volatile("" ::: "memory");
      }
      const float mw = __uint_as_float(gm[0]), lw = __uint_as_float(gm[2]);
      float M = fmaxf(mw, dppf<0x124>(mw)); M = fmaxf(M, dppf<0x122>(M)); M = fmaxf(M, dppf<0x121>(M));
      const float e = __builtin_amdgcn_exp2f(mw - M);
      float L = e * lw; L += dppf<0x124>(L); L += dppf<0x122>(L); L += dppf<0x121>(L);
      const float sv = e / L;
#pragma unroll
      for (int s = 0; s < 4; ++s) scl[s] = __builtin_bit_cast(float, __builtin_amdgcn_readlane(__builtin_bit_cast(int, sv), half * 4 + s));
      STAMP(3);
    }
    wg_barrier();
    if (wave == 4) {
      const float* op = (const float*)(lds + L_OP) + 2 * lane;
      f32x2 o = *(const f32x2*)op * scl[0];
#pragma unroll
      for (int s = 1; s < 4; ++s) o += *(const f32x2*)(op + 128 * s) * scl[s];
      st_gran2(rws, WS_O + h * 8192 + jj * 1024 + lane * 16, o[0], o[1], tag);
      STAMP(4);
    }
  }
  if (role == 3) {
    if (wave >= 4) {
      const int bb = wave - 4;
      u32x4 a0 = {0, ~tag, 0, ~tag}, a1 = a0;
      for (unsigned spins = 0; spins < SPIN_CAP; ++spins) {
        const bool n0 = !(a0[1] == tag && a0[3] == tag), n1 = !(a1[1] == tag && a1[3] == tag);
        if (!__any(n0 || n1)) break;
        if (n0) a0 = bld4<SC1>(rws, WS_O + h * 8192 + bb * 2048 + lane * 16, 0);
        if (n1) a1 = bld4<SC1>(rws, WS_O + h * 8192 + bb * 2048 + lane * 16, 1024);
        asm volatile("" ::: "memory");
      }
      *(unsigned*)(lds + L_ACT + bb * OROW + lane * 4) =
          cvtpk(__uint_as_float(a0[0]) + __uint_as_float(a1[0]), __uint_as_float(a0[2]) + __uint_as_float(a1[2]));
    }
    STAMP(5);
    wg_barrier();
    if (wave < 5) {
      f32x4 y0 = {0, 0, 0, 0}, y1 = {0, 0, 0, 0};
      const unsigned char* wl = lds + L_WT + (8 * g + (c >> 2)) * 320 + wave * 64 + (c & 3) * 8;
#pragma unroll
      for (int t = 0; t < 4; ++t) {
        const u32x4 ob = *(const u32x4*)(lds + L_ACT + (c & 3) * OROW + t * 64 + g * 16);
        y0 = mfma(trfrag(wl + t * 32 * 320, 4 * 320), ob, y0);
        y1 = mfma(trfrag(wl + t * 32 * 320 + 32, 4 * 320), ob, y1);
      }
      if (c < 4) {
        const int cs = b * 10 + wave * 2;
        const int off = WS_Y + ((c * 40 + cs) * 32 + h) * 128 + g * 32;
        st_gran2(rws, off, y0[0], y0[1], tag); st_gran2(rws, off + 16, y0[2], y0[3], tag);
        st_gran2(rws, off + 4096, y1[0], y1[1], tag); st_gran2(rws, off + 4096 + 16, y1[2], y1[3], tag);
      }
      STAMP(6);
    } else {
      if (wave == 7 && b == 0 && lane == 0) __builtin_amdgcn_raw_buffer_store_b64(u32x2{0u, tag}, rws, WS_FLAG + h * 8, 0, SC1);
      const int task = ridx + 128 * (wave - 5);
      if (task < 160) {
        const int rb = task / 40, cs = task % 40;
        const unsigned xv = *(const unsigned*)(x + rb * D + 16 * cs + 2 * (lane & 7));
        u32x4 y[4];
#pragma unroll
        for (int i = 0; i < 4; ++i) y[i] = u32x4{0, ~tag, 0, ~tag};
        for (unsigned spins = 0; spins < SPIN_CAP; ++spins) {
          bool n[4], any = false;
#pragma unroll
          for (int i = 0; i < 4; ++i) { n[i] = !(y[i][1] == tag && y[i][3] == tag); any = any || n[i]; }
          if (!__any(any)) break;
#pragma unroll
          for (int i = 0; i < 4; ++i) if (n[i]) y[i] = bld4<SC1>(rws, WS_Y + task * 4096 + lane * 16, i * 1024);
          asm volatile("" ::: "memory");
        }
        STAMP(7);
        float s0 = 0.f, s1 = 0.f;
#pragma unroll
        for (int i = 0; i < 4; ++i) { s0 += __uint_as_float(y[i][0]); s1 += __uint_as_float(y[i][2]); }
        s0 += dppf<0x128>(s0); s1 += dppf<0x128>(s1);
        s0 = x32_sum(x16_sum(s0)); s1 = x32_sum(x16_sum(s1));
        const unsigned hv = cvtpk(bf2f(xv & 0xffff) + rbf(s0), bfhi(xv) + rbf(s1));
        if (lane < 8) __builtin_amdgcn_raw_buffer_store_b64(u32x2{hv, tag}, rws, WS_H + (rb * 320 + 8 * cs + lane) * 8, 0, SC1);
        STAMP(8);
      }
    }
  } else if (role == 1) {
    wg_barrier();
    {
      u32x4 wt[10];
      const rsrc_t rG = mkrs(wg, D * F * 2), rU = mkrs(wu, D * F * 2);
      const int vo = (tid >> 2) * (F * 2) + (tid & 3) * 16 + ridx * 64;
#pragma unroll
      for (int n = 0; n < 5; ++n) { wt[2 * n] = bld4<TAUX>(rG, vo, n * 128 * F * 2); wt[2 * n + 1] = bld4<TAUX>(rU, vo, n * 128 * F * 2); }
      if (tid < 80) *(u32x4*)(lds + L_ACT + 4 * HROW + tid * 16) = u32x4{0x3f803f80u, 0x3f803f80u, 0x3f803f80u, 0x3f803f80u};
#pragma unroll
      for (int n = 0; n < 5; ++n) {
        *(u32x4*)(lds + L_WT + n * 8192 + tid * 16) = wt[2 * n];
        *(u32x4*)(lds + L_WT + 40960 + n * 8192 + tid * 16) = wt[2 * n + 1];
      }
    }
    wg_barrier();
    const int kq = wave & 3, ct = wave >> 2;
    u32x4 af[10];
    {
      const unsigned char* wl = lds + L_WT + (kq * 160 + 8 * g + (c >> 2)) * 64 + ct * 32 + (c & 3) * 8;
#pragma unroll
      for (int t = 0; t < 5; ++t) { af[2 * t] = trfrag(wl + t * 2048, 256); af[2 * t + 1] = trfrag(wl + 40960 + t * 2048, 256); }
#pragma unroll
      for (int t = 0; t < 10; ++t) asm volatile("" : "+v"(af[t]));
    }
    STAMP(6);
    if (wave == 0) {
    for (unsigned spins = 0; spins < SPIN_CAP; ++spins) {
      const u32x4 f = bld4<SC1>(rws, WS_FLAG + (lane & 15) * 16, 0);
      if (__all(f[1] == tag && f[3] == tag)) break;
      __builtin_amdgcn_s_sleep(2);
      asm volatile("" ::: "memory");
    }
    }
    wg_barrier();
    STAMP(7);
    {
      const int p0 = tid, p1 = 512 + (tid & 127);
      u32x4 a0, a1;
      const bool two = wave >= 6;
      for (unsigned spins = 0; spins < SPIN_CAP; ++spins) {
        a0 = bld4<SC1>(rws, WS_H + p0 * 16, 0);
        bool ok = true;
        if (two) { a1 = bld4<SC1>(rws, WS_H + p1 * 16, 0); ok = a1[1] == tag && a1[3] == tag; }
        ok = ok && a0[1] == tag && a0[3] == tag;
        if (__all(ok)) break;
        asm volatile("" ::: "memory");
      }
      *(u32x2*)(lds + L_ACT + (p0 / 160) * HROW + (p0 % 160) * 8) = u32x2{a0[0], a0[2]};
      if (two) *(u32x2*)(lds + L_ACT + (p1 / 160) * HROW + (p1 % 160) * 8) = u32x2{a1[0], a1[2]};
    }
    STAMP(8);
    wg_barrier();
    {
      const int row = c == 4 ? 4 : (c & 3);
      f32x4 aG = {0, 0, 0, 0}, aU = {0, 0, 0, 0}, aS = {0, 0, 0, 0};
#pragma unroll
      for (int t = 0; t < 5; ++t) {
        const u32x4 hb = *(const u32x4*)(lds + L_ACT + row * HROW + (kq * 5 + t) * 64 + g * 16);
        aG = mfma(af[2 * t], hb, aG);
        aU = mfma(af[2 * t + 1], hb, aU);
        if (ct == 0) aS = mfma(hb, hb, aS);
      }
      if (c < 5) {
        float* rd = (float*)(lds + L_RED) + wave * 240 + c * 16 + 4 * g;
        *(f32x4*)(rd) = aG; *(f32x4*)(rd + 80) = aU;
        if (ct == 0) *(f32x4*)(rd + 160) = aS;
      }
    }
    wg_barrier();
    if (wave < 2) {
      const int bb = g, i = c;
      const float* rd = (const float*)(lds + L_RED);
      const float* rt = rd + wave * 960;
      float G = 0.f, U = 0.f, cG = 0.f, cU = 0.f, S1 = 0.f, S2 = 0.f;
#pragma unroll
      for (int w = 0; w < 4; ++w) {
        G += rt[w * 240 + bb * 16 + i]; cG += rt[w * 240 + 64 + i];
        U += rt[w * 240 + 80 + bb * 16 + i]; cU += rt[w * 240 + 80 + 64 + i];
        S2 += rd[w * 240 + 160 + bb * 16 + bb]; S1 += rd[w * 240 + 160 + bb * 16 + 4];
      }
      const float mean = S1 * (1.f / D);
      const float var = fmaxf(S2 * (1.f / D) - mean * mean, 0.f);
      const float mb = rbf(mean), rs = 1.f / rbf(sqrtf(rbf(rbf(var) + EPS)));
      const float gate = rbf((G - mb * cG) * rs), up = rbf((U - mb * cU) * rs);
      const float gv = rbf(gelu_tanh(gate)) * up;
      const float gn = dppf<0xB1>(gv);
      const unsigned pk = cvtpk(gv, gn);
      if (!(lane & 1)) __builtin_amdgcn_raw_buffer_store_b64(u32x2{pk, tag}, rws, WS_G + (ridx * 2 + wave) * 256 + (lane >> 1) * 8, 0, SC1);
      STAMP(9);
    }
  } else if (role == 2) {
    wg_barrier();
    {
      u32x4 wt[7];
      const rsrc_t rD = mkrs(wd, F * D * 2);
      const int vo = (tid >> 1) * (D * 2) + (tid & 1) * 16 + ridx * 32;
#pragma unroll
      for (int n = 0; n < 7; ++n) wt[n] = bld4<TAUX>(rD, vo + n * 256 * D * 2, 0);
      if (tid < 32) *(u32x4*)(lds + L_ACT + (tid >> 3) * GROW + 3456 + (tid & 7) * 16) = u32x4{0, 0, 0, 0};
#pragma unroll
      for (int n = 0; n < 7; ++n) *(u32x4*)(lds + L_WT + n * 8192 + tid * 16) = wt[n];
    }
    u32x4 af[7];
#pragma unroll
    for (int n = 0; n < 7; ++n) af[n] = trfrag(lds + L_WT + n * 8192 + wave * 1024 + g * 256 + c * 8, 128);
#pragma unroll
    for (int n = 0; n < 7; ++n) asm volatile("" : "+v"(af[n]));
    float hres = 0.f;
    STAMP(6);
    if (wave == 0) {
    for (unsigned spins = 0; spins < SPIN_CAP; ++spins) {
      const u32x4 f = bld4<SC1>(rws, WS_FLAG + (lane & 15) * 16, 0);
      if (__all(f[1] == tag && f[3] == tag)) break;
      __builtin_amdgcn_s_sleep(2);
      asm volatile("" ::: "memory");
    }
      u32x2 hv;
      for (unsigned spins = 0; spins < SPIN_CAP; ++spins) {
        hv = bld2<SC1>(rws, WS_H + (g * 320 + 8 * ridx + (c >> 1)) * 8, 0);
        if (__all(hv[1] == tag)) break;
        __builtin_amdgcn_s_sleep(2);
        asm volatile("" ::: "memory");
      }
      hres = (c & 1) ? bfhi(hv[0]) : bf2f(hv[0] & 0xffff);
    }
    wg_barrier();
    STAMP(7);
    {
      u32x4 a[4];
      const bool four = wave >= 5;
      const int p3 = 1536 + (tid - 320);
      for (unsigned spins = 0; spins < SPIN_CAP; ++spins) {
#pragma unroll
        for (int i = 0; i < 3; ++i) a[i] = bld4<SC1>(rws, WS_G + tid * 16, i * 8192);
        bool ok = true;
        if (four) { a[3] = bld4<SC1>(rws, WS_G + p3 * 16, 0); ok = a[3][1] == tag && a[3][3] == tag; }
#pragma unroll
        for (int i = 0; i < 3; ++i) ok = ok && a[i][1] == tag && a[i][3] == tag;
        if (__all(ok)) break;
        asm volatile("" ::: "memory");
      }
#pragma unroll
      for (int i = 0; i < 4; ++i) {
        const int p = i < 3 ? tid + 512 * i : p3;
        if (i < 3 || four) *(u32x2*)(lds + L_ACT + ((p >> 2) & 3) * GROW + (p >> 4) * 32 + (p & 3) * 8) = u32x2{a[i][0], a[i][2]};
      }
    }
    STAMP(8);
    wg_barrier();
    {
      f32x4 a0 = {0, 0, 0, 0}, a1 = {0, 0, 0, 0};
#pragma unroll
      for (int n = 0; n < 7; ++n) {
        const u32x4 gb = *(const u32x4*)(lds + L_ACT + (c & 3) * GROW + (n * 8 + wave) * 64 + g * 16);
        if (n & 1) a1 = mfma(af[n], gb, a1); else a0 = mfma(af[n], gb, a0);
      }
      a0 += a1;
      if (c < 4) *(f32x4*)((float*)(lds + L_RED) + wave * 64 + c * 16 + 4 * g) = a0;
    }
    wg_barrier();
    if (wave == 0) {
      const float* rd = (const float*)(lds + L_RED) + lane;
      const float y = ((rd[0] + rd[64]) + (rd[128] + rd[192])) + ((rd[256] + rd[320]) + (rd[384] + rd[448]));
      const float r = hres + rbf(y);
      const float rn = dppf<0xB1>(r);
      if (!(lane & 1)) *(unsigned*)(out + g * D + 16 * ridx + c) = cvtpk(r, rn);
      if (ridx == 0 && lane == 0) __builtin_amdgcn_raw_buffer_store_b32(tag, rws, WS_EPOCH, 0, SC1);
      STAMP(9);
    }
  }
  STAMP_FLUSH;
}

extern "C" void kernel_launch(void* const* d_in, const int* in_sizes, int n_in, void* d_out, int out_size, void* d_ws, size_t ws_size,
                              hipStream_t stream) {
  fused_kernel<<<256, 512, 0, stream>>>((const u16*)d_in[0], (const u16*)d_in[1], (const u16*)d_in[2], (const u16*)d_in[3], (const u16*)d_in[4],
                                        (const u16*)d_in[6], (const u16*)d_in[7], (const u16*)d_in[8], (u16*)d_out, (unsigned char*)d_ws);
}
```

```cpp
#include <hip/hip_runtime.h>
#include <stdint.h>
#include <type_traits>

#define STAMPS 1
#define DIAG 0
#define DSTEADY 5
#define DTAIL 10
#define TAUX 2
#define WAUX 0

typedef unsigned u32x4 __attribute__((ext_vector_type(4)));
typedef unsigned u32x2 __attribute__((ext_vector_type(2)));
typedef float f32x4 __attribute__((ext_vector_type(4)));
typedef float f32x2 __attribute__((ext_vector_type(2)));
typedef __bf16 bf16x8 __attribute__((ext_vector_type(8)));
typedef __bf16 bf16x2 __attribute__((ext_vector_type(2)));
typedef short s16x4 __attribute__((ext_vector_type(4)));
typedef unsigned short u16;
typedef unsigned long long u64;
#define LAS __attribute__((address_space(3)))

constexpr int D = 640, H = 32, SK = 4096, DH = 128, F = 1728, HD = H * DH;
constexpr float EPS = 1e-5f;

constexpr int WS_EPOCH = 0;
constexpr int WS_ML = 4096;
constexpr int WS_H = 32768;
constexpr int WS_G = 65536;
constexpr int WS_O = 131072;
constexpr int WS_Y = WS_O + 32 * 8192;
constexpr int WS_Q = WS_Y + 160 * 4096;
constexpr int WS_FLAG = 1024;
constexpr int WS_BYTES = WS_Q + 128 * 512;
constexpr int WS_STAMP = 4 << 20;

constexpr int L_Q = 0;
constexpr int L_OP = 256;
constexpr int L_RED = 2304;
constexpr int L_ACT = L_RED + 8192;
constexpr int OROW = 256 + 16, HROW = 1280 + 32, GROW = 3584 + 32;
constexpr int L_WT = L_ACT + 4 * GROW + 64;
constexpr int L_BYTES = L_WT + 81920;
static_assert(L_WT % 16 == 0 && L_ACT % 16 == 0, "align");
#define PPRE 32
#define CUR 6

__device__ __forceinline__ float bf2f(unsigned v) { return __builtin_bit_cast(float, v << 16); }
__device__ __forceinline__ float bfhi(unsigned v) { return __builtin_bit_cast(float, v & 0xffff0000u); }
__device__ __forceinline__ unsigned cvtpk(float lo, float hi) {
  bf16x2 r = {(__bf16)lo, (__bf16)hi};
  return __builtin_bit_cast(unsigned, r);
}
__device__ __forceinline__ float rbf(float x) { return (float)(__bf16)x; }
template <int CTRL> __device__ __forceinline__ float dppf(float x) {
  return __builtin_bit_cast(float, __builtin_amdgcn_mov_dpp(__builtin_bit_cast(int, x), CTRL, 0xf, 0xf, true));
}
template <int N> __device__ __forceinline__ unsigned bcast(unsigned x) { return __builtin_amdgcn_mov_dpp(x, 0x150 + N, 0xf, 0xf, true); }
__device__ __forceinline__ float row_sum(float x) {
  x += dppf<0x128>(x); x += dppf<0x124>(x); x += dppf<0x122>(x); x += dppf<0x121>(x); return x;
}
__device__ __forceinline__ float row_max(float x) {
  x = fmaxf(x, dppf<0x128>(x)); x = fmaxf(x, dppf<0x124>(x)); x = fmaxf(x, dppf<0x122>(x)); x = fmaxf(x, dppf<0x121>(x)); return x;
}
__device__ __forceinline__ float x16_sum(float x) {
  auto s = __builtin_amdgcn_permlane16_swap(__float_as_uint(x), __float_as_uint(x), false, false);
  return __uint_as_float(s[0]) + __uint_as_float(s[1]);
}
__device__ __forceinline__ float x32_sum(float x) {
  auto t = __builtin_amdgcn_permlane32_swap(__float_as_uint(x), __float_as_uint(x), false, false);
  return __uint_as_float(t[0]) + __uint_as_float(t[1]);
}
__device__ __forceinline__ float xrow_max(float x) {
  auto s = __builtin_amdgcn_permlane16_swap(__float_as_uint(x), __float_as_uint(x), false, false);
  x = fmaxf(__uint_as_float(s[0]), __uint_as_float(s[1]));
  auto t = __builtin_amdgcn_permlane32_swap(__float_as_uint(x), __float_as_uint(x), false, false);
  return fmaxf(__uint_as_float(t[0]), __uint_as_float(t[1]));
}
__device__ __forceinline__ float wave_sum(float x) { return x32_sum(x16_sum(row_sum(x))); }
__device__ __forceinline__ float wave_max(float x) { return xrow_max(row_max(x)); }
__device__ __forceinline__ float dot2(unsigned a, unsigned b, float c) {
  return __builtin_amdgcn_fdot2_f32_bf16(__builtin_bit_cast(bf16x2, a), __builtin_bit_cast(bf16x2, b), c, false);
}
__device__ __forceinline__ f32x4 mfma(u32x4 a, u32x4 b, f32x4 c) {
  return __builtin_amdgcn_mfma_f32_16x16x32_bf16(__builtin_bit_cast(bf16x8, a), __builtin_bit_cast(bf16x8, b), c, 0, 0, 0);
}
template <int I> __device__ __forceinline__ u32x4 selfrag(unsigned vdup, const u32x4& m) {
  unsigned t = bcast<I>(vdup);
  return u32x4{t & m[0], t & m[1], t & m[2], t & m[3]};
}
template <int I, int N, class Fn> __device__ __forceinline__ void static_for(Fn&& f) {
  if constexpr (I < N) { f(std::integral_constant<int, I>{}); static_for<I + 1, N>(f); }
}
typedef __amdgpu_buffer_rsrc_t rsrc_t;
__device__ __forceinline__ rsrc_t mkrs(const void* p, unsigned bytes) { return __builtin_amdgcn_make_buffer_rsrc((void*)p, 0, bytes, 0x00020000); }
template <int AUX> __device__ __forceinline__ u32x4 bld4(rsrc_t r, int voff, int soff) { return __builtin_amdgcn_raw_buffer_load_b128(r, voff, soff, AUX); }
template <int AUX> __device__ __forceinline__ u32x2 bld2(rsrc_t r, int voff, int soff) { return __builtin_amdgcn_raw_buffer_load_b64(r, voff, soff, AUX); }
constexpr int SC1 = 16, NT = 2;
__device__ __forceinline__ void wg_barrier() {
  asm volatile("s_waitcnt lgkmcnt(0)" ::: "memory");
  __builtin_amdgcn_s_barrier();
  asm volatile("" ::: "memory");
}
constexpr unsigned SPIN_CAP = 1u << 20;

__device__ __forceinline__ float gelu_tanh(float x) {
  float u = 0.7978845608028654f * (x + 0.044715f * x * x * x);
  float e = __builtin_amdgcn_exp2f(u * 2.885390081777927f);
  float t = 1.f - 2.f * __builtin_amdgcn_rcpf(1.f + e);
  return 0.5f * x * (1.f + t);
}

#define STAMP(k) do { st_[k] = (unsigned)__builtin_amdgcn_s_memrealtime(); } while (0)
#define STAMP_DECL unsigned st_[12] = {0, 0, 0, 0, 0, 0, 0, 0, 0, 0, 0, 0}
#define STAMP_FLUSH do { if (DIAG >= 0) { if (role == 2 && ridx == 0 && wave == 0) { unsigned tgt = st_[DIAG < 0 ? 0 : DIAG] + 10000u; while ((int)((unsigned)__builtin_amdgcn_s_memrealtime() - tgt) < 0) __builtin_amdgcn_s_sleep(1); } } else if (lane == 0) { _Pragma("unroll") for (int k_ = 0; k_ < 12; ++k_) ((u64*)(ws + WS_STAMP))[(bid * 8 + wave) * 16 + k_] = st_[k_] ? (u64)st_[k_] + (1ull << 32) : 0; } } while (0)

__device__ __forceinline__ u32x4 trfrag(const unsigned char* p, int rowstride4) {
  s16x4 lo = __builtin_amdgcn_ds_read_tr16_b64_v4i16((LAS s16x4*)p);
  s16x4 hi = __builtin_amdgcn_ds_read_tr16_b64_v4i16((LAS s16x4*)(p + rowstride4));
  u32x2 l2 = __builtin_bit_cast(u32x2, lo), h2 = __builtin_bit_cast(u32x2, hi);
  return u32x4{l2[0], l2[1], h2[0], h2[1]};
}
__device__ __forceinline__ void st_gran2(rsrc_t rws, int off, float a, float b, unsigned tag) {
  __builtin_amdgcn_raw_buffer_store_b128(u32x4{__float_as_uint(a), tag, __float_as_uint(b), tag}, rws, off, 0, SC1);
}
constexpr int k_issued(int i) {
  int a = PPRE + DSTEADY + i / CUR, b = i + DSTEADY + 1;
  return a > b ? a : b;
}

__global__ __launch_bounds__(512, 2) void fused_kernel(const u16* __restrict__ x, const u16* __restrict__ kc, const u16* __restrict__ vc,
                                                       const u16* __restrict__ wq, const u16* __restrict__ wo, const u16* __restrict__ wg,
                                                       const u16* __restrict__ wu, const u16* __restrict__ wd, u16* __restrict__ out,
                                                       unsigned char* ws) {
  __shared__ __attribute__((aligned(16))) unsigned char lds[L_BYTES];
  const int bid = blockIdx.x;
  const int xcd = bid & 7, slot = bid >> 3;
  const int h = xcd * 4 + (slot >> 3), b = (slot >> 1) & 3, half = slot & 1, jj = slot & 7;
  const int tid = threadIdx.x, lane = tid & 63, wave = __builtin_amdgcn_readfirstlane(tid >> 6);
  const int g = lane >> 4, c = lane & 15;
  STAMP_DECL;
  STAMP(0);
  int role = 0, ridx = 0;
  {
    const int i2 = slot >> 1;
    if (!half) { role = 3; ridx = i2 * 8 + xcd; }
    else if (i2 < 8) { int pr = (i2 >> 1) * 8 + xcd; if (pr < 27) { role = 1; ridx = pr * 2 + (i2 & 1); } }
    else { int grp = ((i2 - 8) >> 2) * 8 + xcd; if (grp < 10) { role = 2; ridx = grp * 4 + (i2 & 3); } }
  }
  const rsrc_t rws = mkrs(ws, WS_BYTES);
  unsigned tag = __builtin_amdgcn_raw_buffer_load_b32(rws, WS_EPOCH, 0, SC1);

  if (wave < 4) {
    u32x4 msk;
#pragma unroll
    for (int r = 0; r < 4; ++r) msk[r] = (c < 8 && (c >> 1) == r) ? ((c & 1) ? 0xffff0000u : 0x0000ffffu) : 0u;
    const size_t kvoff = ((size_t)(b * H + h) * SK + half * 2048) * DH;
    const rsrc_t rk = mkrs(kc + kvoff, 512 * 1024), rv = mkrs(vc + kvoff, 512 * 1024);
    const int voff = wave * 1024 + lane * 16;
    constexpr int DS = DSTEADY, DT = DTAIL;
    u32x4 k0[64 + DS];
    static_for<0, DS>([&](auto J) { k0[J.value] = bld4<NT>(rk, voff, J.value * 4096); });
    __builtin_amdgcn_sched_barrier(0);
    static_for<0, PPRE>([&](auto I) {
      constexpr int i = I.value;
      asm volatile("" : "+v"(k0[i]));
      __builtin_amdgcn_sched_barrier(0);
      k0[i + DS] = bld4<NT>(rk, voff, (i + DS) * 4096);
      __builtin_amdgcn_sched_barrier(0);
    });
    STAMP(10);
    wg_barrier();
    const u32x4 q = *(const u32x4*)(lds + L_Q + 16 * c);
    asm volatile("v_add_u32 %0, 1, %0" : "+v"(tag));
    STAMP(1);
    float sc[8];
    auto kstep = [&](u32x4 k) -> float {
      float d = dot2(k[0], q[0], 0.f);
      d = dot2(k[1], q[1], d); d = dot2(k[2], q[2], d); d = dot2(k[3], q[3], d);
      return row_sum(d);
    };
    {
      float kk[4] = {0.f, 0.f, 0.f, 0.f};
      static_for<0, 64>([&](auto I) {
        constexpr int i = I.value;
        constexpr int lo = i == 0 ? PPRE + DS : k_issued(i - 1), hi = k_issued(i);
        if constexpr (hi > lo) {
          asm volatile("" : "+v"(kk[0]), "+v"(kk[1]), "+v"(kk[2]), "+v"(kk[3]));
          static_for<lo, hi>([&](auto J) {
            constexpr int j = J.value;
            asm volatile("" : "+v"(k0[j - DS]));
            __builtin_amdgcn_sched_barrier(0);
            k0[j] = bld4<NT>(rk, voff, j * 4096);
            __builtin_amdgcn_sched_barrier(0);
          });
        }
        float d = kstep(k0[i]);
        kk[i >> 4] = (c == (i & 15)) ? d : kk[i >> 4];
      });
      static_assert(k_issued(63) == 64 + DS, "catch-up must finish inside the peeled part");
      sc[4] = kk[0]; sc[5] = kk[1]; sc[6] = kk[2]; sc[7] = kk[3];
    }
    u32x4 ring[32];
#pragma unroll
    for (int j = 0; j < DS; ++j) ring[j] = k0[64 + j];
#pragma unroll 1
    for (int it = 2; it < 4; ++it) {
#pragma unroll
      for (int i = 0; i < 6; ++i) sc[i] = sc[i + 2];
      const rsrc_t rn = it == 3 ? rv : rk;
      const int so = it * 32 * 4096, son = it == 3 ? 0 : so + 32 * 4096;
      float ka = 0.f, kb = 0.f;
      static_for<0, 32>([&](auto I) {
        constexpr int i = I.value;
        u32x4 cur = ring[i];
        asm volatile("" : "+v"(cur));
        __builtin_amdgcn_sched_barrier(0);
        if constexpr (i + DS < 32) ring[i + DS] = bld4<NT>(rk, voff, so + (i + DS) * 4096);
        else ring[i + DS - 32] = bld4<NT>(rn, voff, son + (i + DS - 32) * 4096);
        float d = kstep(cur);
        if constexpr (i < 16) { ka = (c == i) ? d : ka; asm volatile("" : "+v"(ka)); } else { kb = (c == i - 16) ? d : kb; asm volatile("" : "+v"(kb)); }
        __builtin_amdgcn_sched_barrier(0);
      });
      sc[6] = ka; sc[7] = kb;
    }
    STAMP(2);
    constexpr float SCALE = 0.08838834764831845f, LOG2E = 1.4426950408889634f;
    float m = -INFINITY;
#pragma unroll
    for (int i = 0; i < 8; ++i) { sc[i] = rbf(rbf(sc[i]) * SCALE) * LOG2E; m = fmaxf(m, sc[i]); }
    m = wave_max(m);
    unsigned pd[8];
    float l = 0.f;
#pragma unroll
    for (int i = 0; i < 8; ++i) { float p = __builtin_amdgcn_exp2f(sc[i] - m); pd[i] = cvtpk(p, p); l += bfhi(pd[i]); }
    l = wave_sum(l);
    if (lane == 0) st_gran2(rws, WS_ML + (b * H + h) * 128 + half * 64 + wave * 16, m, l, tag);
    f32x4 acc0 = {0, 0, 0, 0}, acc1 = {0, 0, 0, 0};
#pragma unroll 1
    for (int it = 0; it < 3; ++it) {
      const int so = it * 32 * 4096;
      static_for<0, 32>([&](auto I) {
        constexpr int i = I.value;
        u32x4 a = selfrag<i & 15>(pd[i >> 4], msk);
        u32x4 cur = ring[i];
        asm volatile("" : "+v"(cur));
        __builtin_amdgcn_sched_barrier(0);
        ring[(i + DS) % 32] = bld4<NT>(rv, voff, so + (i + DS) * 4096);
        if constexpr (i & 1) { acc1 = mfma(a, cur, acc1); asm volatile("" : "+v"(acc1)); } else { acc0 = mfma(a, cur, acc0); asm volatile("" : "+v"(acc0)); }
        __builtin_amdgcn_sched_barrier(0);
      });
#pragma unroll
      for (int i = 0; i < 6; ++i) pd[i] = pd[i + 2];
    }
    u32x4 wt[10];
    {
      const int so = 3 * 32 * 4096;
      const rsrc_t rW = mkrs(wo, role == 3 ? HD * D * 2 : 0);
#pragma unroll
      for (int j = DS; j < DT; ++j) ring[j] = bld4<NT>(rv, voff, so + j * 4096);
      static_for<0, 32>([&](auto I) {
        constexpr int i = I.value;
        u32x4 a = selfrag<i & 15>(pd[i >> 4], msk);
        u32x4 cur = ring[i];
        asm volatile("" : "+v"(cur));
        __builtin_amdgcn_sched_barrier(0);
        if constexpr (i + DT < 32) ring[i + DT] = bld4<NT>(rv, voff, so + (i + DT) * 4096);
        if constexpr (i + DT == 32) {
#pragma unroll
          for (int n = 0; n < 10; ++n) {
            const int p = n * 64 + lane, row = p / 20, ch = p - row * 20;
            wt[n] = bld4<TAUX>(rW, row * (D * 2) + ch * 16, (h * DH + wave * 32) * (D * 2) + b * 320);
          }
        }
        if constexpr (i & 1) { acc1 = mfma(a, cur, acc1); asm volatile("" : "+v"(acc1)); } else { acc0 = mfma(a, cur, acc0); asm volatile("" : "+v"(acc0)); }
        __builtin_amdgcn_sched_barrier(0);
      });
    }
    acc0 += acc1;
    if (lane < 32) *(f32x4*)(lds + L_OP + (wave * 128 + 8 * c + 4 * g) * 4) = acc0;
    STAMP(3);
    wg_barrier();
    if (role == 3) {
#pragma unroll
      for (int n = 0; n < 10; ++n) *(u32x4*)(lds + L_WT + wave * 10240 + n * 1024 + lane * 16) = wt[n];
    }
  } else {
    float scl[4] = {0.f, 0.f, 0.f, 0.f};
    if (wave == 4) {
      u32x4 xs[5];
#pragma unroll
      for (int t = 0; t < 5; ++t) xs[t] = *(const u32x4*)(x + g * D + c * 40 + t * 8);
      __builtin_amdgcn_sched_barrier(0);
      u32x4 wf[20];
      {
        const rsrc_t rq = mkrs(wq, D * HD * 2);
        const int vo = (lane >> 1) * HD * 2 + (lane & 1) * 16;
        const int so = (h * DH + 16 * jj) * 2;
#pragma unroll
        for (int i = 0; i < 20; ++i) wf[i] = bld4<NT>(rq, vo, so + i * 32 * HD * 2);
      }
      __builtin_amdgcn_sched_barrier(0);
      asm volatile("v_add_u32 %0, 1, %0" : "+v"(tag));
      float s = 0.f;
#pragma unroll
      for (int t = 0; t < 5; ++t)
#pragma unroll
        for (int r = 0; r < 4; ++r) s += bf2f(xs[t][r] & 0xffff) + bfhi(xs[t][r]);
      const float mean = row_sum(s) * (1.f / D);
      float v = 0.f;
#pragma unroll
      for (int t = 0; t < 5; ++t)
#pragma unroll
        for (int r = 0; r < 4; ++r) { float a = bf2f(xs[t][r] & 0xffff) - mean, bb = bfhi(xs[t][r]) - mean; v += a * a + bb * bb; }
      const float var = row_sum(v) * (1.f / D);
      const float mb = rbf(mean), rs = 1.f / rbf(sqrtf(rbf(rbf(var) + EPS)));
#pragma unroll
      for (int t = 0; t < 5; ++t) {
        u32x4 xn;
#pragma unroll
        for (int r = 0; r < 4; ++r) {
          const unsigned d = cvtpk(bf2f(xs[t][r] & 0xffff) - mb, bfhi(xs[t][r]) - mb);
          xn[r] = cvtpk(bf2f(d & 0xffff) * rs, bfhi(d) * rs);
        }
        *(u32x4*)(lds + L_ACT + g * HROW + c * 80 + t * 16) = xn;
      }
      f32x4 a0 = {0, 0, 0, 0}, a1 = {0, 0, 0, 0};
#pragma unroll
      for (int i = 0; i < 20; ++i) *(u32x4*)(lds + L_WT + i * 1024 + lane * 16) = wf[i];
#pragma unroll
      for (int i = 0; i < 20; ++i) {
        const u32x4 af = trfrag(lds + L_WT + i * 1024 + g * 256 + c * 8, 128);
        const u32x4 xb = *(const u32x4*)(lds + L_ACT + (c & 3) * HROW + i * 64 + g * 16);
        if (i & 1) a1 = mfma(af, xb, a1); else a0 = mfma(af, xb, a0);
      }
      a0 += a1;
      if (c < 4)
        __builtin_amdgcn_raw_buffer_store_b128(u32x4{cvtpk(a0[0], a0[1]), tag, cvtpk(a0[2], a0[3]), tag}, rws,
                                               WS_Q + (h * 4 + c) * 512 + jj * 64 + g * 16, 0, SC1);
      STAMP(10);
      u32x4 qv;
      for (unsigned spins = 0; spins < SPIN_CAP; ++spins) {
        qv = bld4<SC1>(rws, WS_Q + (h * 4 + b) * 512 + (lane & 31) * 16, 0);
        if (__all(qv[1] == tag && qv[3] == tag)) break;
        __builtin_amdgcn_s_sleep(4);
        asm volatile("" ::: "memory");
      }
      if (lane < 32) *(u32x2*)(lds + L_Q + lane * 8) = u32x2{qv[0], qv[2]};
      STAMP(1);
    } else {
      asm volatile("v_add_u32 %0, 1, %0" : "+v"(tag));
    }
    wg_barrier();
    if (wave == 4) {
      u32x4 gm;
      for (unsigned spins = 0; spins < SPIN_CAP; ++spins) {
        __builtin_amdgcn_s_sleep(64);
        gm = bld4<SC1>(rws, WS_ML + (b * H + h) * 128 + (lane & 7) * 16, 0);
        const bool ok = gm[1] == tag && gm[3] == tag;
        if (__all(ok)) break;
        asm volatile("" ::: "memory");
      }
      const float mw = __uint_as_float(gm[0]), lw = __uint_as_float(gm[2]);
      float M = fmaxf(mw, dppf<0x124>(mw)); M = fmaxf(M, dppf<0x122>(M)); M = fmaxf(M, dppf<0x121>(M));
      const float e = __builtin_amdgcn_exp2f(mw - M);
      float L = e * lw; L += dppf<0x124>(L); L += dppf<0x122>(L); L += dppf<0x121>(L);
      const float sv = e / L;
#pragma unroll
      for (int s = 0; s < 4; ++s) scl[s] = __builtin_bit_cast(float, __builtin_amdgcn_readlane(__builtin_bit_cast(int, sv), half * 4 + s));
      STAMP(3);
    }
    wg_barrier();
    if (wave == 4) {
      const float* op = (const float*)(lds + L_OP) + 2 * lane;
      f32x2 o = *(const f32x2*)op * scl[0];
#pragma unroll
      for (int s = 1; s < 4; ++s) o += *(const f32x2*)(op + 128 * s) * scl[s];
      st_gran2(rws, WS_O + h * 8192 + jj * 1024 + lane * 16, o[0], o[1], tag);
      STAMP(4);
    }
  }
  if (role == 3) {
    if (wave >= 4) {
      const int bb = wave - 4;
      u32x4 a0 = {0, ~tag, 0, ~tag}, a1 = a0;
      for (unsigned spins = 0; spins < SPIN_CAP; ++spins) {
        const bool n0 = !(a0[1] == tag && a0[3] == tag), n1 = !(a1[1] == tag && a1[3] == tag);
        if (!__any(n0 || n1)) break;
        if (n0) a0 = bld4<SC1>(rws, WS_O + h * 8192 + bb * 2048 + lane * 16, 0);
        if (n1) a1 = bld4<SC1>(rws, WS_O + h * 8192 + bb * 2048 + lane * 16, 1024);
        asm volatile("" ::: "memory");
      }
      *(unsigned*)(lds + L_ACT + bb * OROW + lane * 4) =
          cvtpk(__uint_as_float(a0[0]) + __uint_as_float(a1[0]), __uint_as_float(a0[2]) + __uint_as_float(a1[2]));
    }
    STAMP(5);
    wg_barrier();
    if (wave < 5) {
      f32x4 y0 = {0, 0, 0, 0}, y1 = {0, 0, 0, 0};
      const unsigned char* wl = lds + L_WT + (8 * g + (c >> 2)) * 320 + wave * 64 + (c & 3) * 8;
#pragma unroll
      for (int t = 0; t < 4; ++t) {
        const u32x4 ob = *(const u32x4*)(lds + L_ACT + (c & 3) * OROW + t * 64 + g * 16);
        y0 = mfma(trfrag(wl + t * 32 * 320, 4 * 320), ob, y0);
        y1 = mfma(trfrag(wl + t * 32 * 320 + 32, 4 * 320), ob, y1);
      }
      if (c < 4) {
        const int cs = b * 10 + wave * 2;
        const int off = WS_Y + ((c * 40 + cs) * 32 + h) * 128 + g * 32;
        st_gran2(rws, off, y0[0], y0[1], tag); st_gran2(rws, off + 16, y0[2], y0[3], tag);
        st_gran2(rws, off + 4096, y1[0], y1[1], tag); st_gran2(rws, off + 4096 + 16, y1[2], y1[3], tag);
      }
      STAMP(6);
    } else {
      if (wave == 7 && b == 0 && lane == 0) __builtin_amdgcn_raw_buffer_store_b64(u32x2{0u, tag}, rws, WS_FLAG + h * 8, 0, SC1);
      const int task = ridx + 128 * (wave - 5);
      if (task < 160) {
        const int rb = task / 40, cs = task % 40;
        const unsigned xv = *(const unsigned*)(x + rb * D + 16 * cs + 2 * (lane & 7));
        u32x4 y[4];
#pragma unroll
        for (int i = 0; i < 4; ++i) y[i] = u32x4{0, ~tag, 0, ~tag};
        for (unsigned spins = 0; spins < SPIN_CAP; ++spins) {
          bool n[4], any = false;
#pragma unroll
          for (int i = 0; i < 4; ++i) { n[i] = !(y[i][1] == tag && y[i][3] == tag); any = any || n[i]; }
          if (!__any(any)) break;
#pragma unroll
          for (int i = 0; i < 4; ++i) if (n[i]) y[i] = bld4<SC1>(rws, WS_Y + task * 4096 + lane * 16, i * 1024);
          asm volatile("" ::: "memory");
        }
        STAMP(7);
        float s0 = 0.f, s1 = 0.f;
#pragma unroll
        for (int i = 0; i < 4; ++i) { s0 += __uint_as_float(y[i][0]); s1 += __uint_as_float(y[i][2]); }
        s0 += dppf<0x128>(s0); s1 += dppf<0x128>(s1);
        s0 = x32_sum(x16_sum(s0)); s1 = x32_sum(x16_sum(s1));
        const unsigned hv = cvtpk(bf2f(xv & 0xffff) + rbf(s0), bfhi(xv) + rbf(s1));
        if (lane < 8) __builtin_amdgcn_raw_buffer_store_b64(u32x2{hv, tag}, rws, WS_H + (rb * 320 + 8 * cs + lane) * 8, 0, SC1);
        STAMP(8);
      }
    }
  } else if (role == 1) {
    wg_barrier();
    {
      u32x4 wt[10];
      const rsrc_t rG = mkrs(wg, D * F * 2), rU = mkrs(wu, D * F * 2);
      const int vo = (tid >> 2) * (F * 2) + (tid & 3) * 16 + ridx * 64;
#pragma unroll
      for (int n = 0; n < 5; ++n) { wt[2 * n] = bld4<TAUX>(rG, vo, n * 128 * F * 2); wt[2 * n + 1] = bld4<TAUX>(rU, vo, n * 128 * F * 2); }
      if (tid < 80) *(u32x4*)(lds + L_ACT + 4 * HROW + tid * 16) = u32x4{0x3f803f80u, 0x3f803f80u, 0x3f803f80u, 0x3f803f80u};
#pragma unroll
      for (int n = 0; n < 5; ++n) {
        *(u32x4*)(lds + L_WT + n * 8192 + tid * 16) = wt[2 * n];
        *(u32x4*)(lds + L_WT + 40960 + n * 8192 + tid * 16) = wt[2 * n + 1];
      }
    }
    wg_barrier();
    const int kq = wave & 3, ct = wave >> 2;
    u32x4 af[10];
    {
      const unsigned char* wl = lds + L_WT + (kq * 160 + 8 * g + (c >> 2)) * 64 + ct * 32 + (c & 3) * 8;
#pragma unroll
      for (int t = 0; t < 5; ++t) { af[2 * t] = trfrag(wl + t * 2048, 256); af[2 * t + 1] = trfrag(wl + 40960 + t * 2048, 256); }
#pragma unroll
      for (int t = 0; t < 10; ++t) asm volatile("" : "+v"(af[t]));
    }
    STAMP(6);
    if (wave == 0) {
    for (unsigned spins = 0; spins < SPIN_CAP; ++spins) {
      const u32x4 f = bld4<SC1>(rws, WS_FLAG + (lane & 15) * 16, 0);
      if (__all(f[1] == tag && f[3] == tag)) break;
      __builtin_amdgcn_s_sleep(2);
      asm volatile("" ::: "memory");
    }
    }
    wg_barrier();
    STAMP(7);
    {
      const int p0 = tid, p1 = 512 + (tid & 127);
      u32x4 a0, a1;
      const bool two = wave >= 6;
      for (unsigned spins = 0; spins < SPIN_CAP; ++spins) {
        a0 = bld4<SC1>(rws, WS_H + p0 * 16, 0);
        bool ok = true;
        if (two) { a1 = bld4<SC1>(rws, WS_H + p1 * 16, 0); ok = a1[1] == tag && a1[3] == tag; }
        ok = ok && a0[1] == tag && a0[3] == tag;
        if (__all(ok)) break;
        asm volatile("" ::: "memory");
      }
      *(u32x2*)(lds + L_ACT + (p0 / 160) * HROW + (p0 % 160) * 8) = u32x2{a0[0], a0[2]};
      if (two) *(u32x2*)(lds + L_ACT + (p1 / 160) * HROW + (p1 % 160) * 8) = u32x2{a1[0], a1[2]};
    }
    STAMP(8);
    wg_barrier();
    {
      const int row = c == 4 ? 4 : (c & 3);
      f32x4 aG = {0, 0, 0, 0}, aU = {0, 0, 0, 0}, aS = {0, 0, 0, 0};
#pragma unroll
      for (int t = 0; t < 5; ++t) {
        const u32x4 hb = *(const u32x4*)(lds + L_ACT + row * HROW + (kq * 5 + t) * 64 + g * 16);
        aG = mfma(af[2 * t], hb, aG);
        aU = mfma(af[2 * t + 1], hb, aU);
        if (ct == 0) aS = mfma(hb, hb, aS);
      }
      if (c < 5) {
        float* rd = (float*)(lds + L_RED) + wave * 240 + c * 16 + 4 * g;
        *(f32x4*)(rd) = aG; *(f32x4*)(rd + 80) = aU;
        if (ct == 0) *(f32x4*)(rd + 160) = aS;
      }
    }
    wg_barrier();
    if (wave < 2) {
      const int bb = g, i = c;
      const float* rd = (const float*)(lds + L_RED);
      const float* rt = rd + wave * 960;
      float G = 0.f, U = 0.f, cG = 0.f, cU = 0.f, S1 = 0.f, S2 = 0.f;
#pragma unroll
      for (int w = 0; w < 4; ++w) {
        G += rt[w * 240 + bb * 16 + i]; cG += rt[w * 240 + 64 + i];
        U += rt[w * 240 + 80 + bb * 16 + i]; cU += rt[w * 240 + 80 + 64 + i];
        S2 += rd[w * 240 + 160 + bb * 16 + bb]; S1 += rd[w * 240 + 160 + bb * 16 + 4];
      }
      const float mean = S1 * (1.f / D);
      const float var = fmaxf(S2 * (1.f / D) - mean * mean, 0.f);
      const float mb = rbf(mean), rs = 1.f / rbf(sqrtf(rbf(rbf(var) + EPS)));
      const float gate = rbf((G - mb * cG) * rs), up = rbf((U - mb * cU) * rs);
      const float gv = rbf(gelu_tanh(gate)) * up;
      const float gn = dppf<0xB1>(gv);
      const unsigned pk = cvtpk(gv, gn);
      if (!(lane & 1)) __builtin_amdgcn_raw_buffer_store_b64(u32x2{pk, tag}, rws, WS_G + (ridx * 2 + wave) * 256 + (lane >> 1) * 8, 0, SC1);
      STAMP(9);
    }
  } else if (role == 2) {
    wg_barrier();
    {
      u32x4 wt[7];
      const rsrc_t rD = mkrs(wd, F * D * 2);
      const int vo = (tid >> 1) * (D * 2) + (tid & 1) * 16 + ridx * 32;
#pragma unroll
      for (int n = 0; n < 7; ++n) wt[n] = bld4<TAUX>(rD, vo + n * 256 * D * 2, 0);
      if (tid < 32) *(u32x4*)(lds + L_ACT + (tid >> 3) * GROW + 3456 + (tid & 7) * 16) = u32x4{0, 0, 0, 0};
#pragma unroll
      for (int n = 0; n < 7; ++n) *(u32x4*)(lds + L_WT + n * 8192 + tid * 16) = wt[n];
    }
    u32x4 af[7];
#pragma unroll
    for (int n = 0; n < 7; ++n) af[n] = trfrag(lds + L_WT + n * 8192 + wave * 1024 + g * 256 + c * 8, 128);
#pragma unroll
    for (int n = 0; n < 7; ++n) asm volatile("" : "+v"(af[n]));
    float hres = 0.f;
    STAMP(6);
    if (wave == 0) {
    for (unsigned spins = 0; spins < SPIN_CAP; ++spins) {
      const u32x4 f = bld4<SC1>(rws, WS_FLAG + (lane & 15) * 16, 0);
      if (__all(f[1] == tag && f[3] == tag)) break;
      __builtin_amdgcn_s_sleep(2);
      asm volatile("" ::: "memory");
    }
      u32x2 hv;
      for (unsigned spins = 0; spins < SPIN_CAP; ++spins) {
        hv = bld2<SC1>(rws, WS_H + (g * 320 + 8 * ridx + (c >> 1)) * 8, 0);
        if (__all(hv[1] == tag)) break;
        __builtin_amdgcn_s_sleep(2);
        asm volatile("" ::: "memory");
      }
      hres = (c & 1) ? bfhi(hv[0]) : bf2f(hv[0] & 0xffff);
    }
    wg_barrier();
    STAMP(7);
    {
      u32x4 a[4];
      const bool four = wave >= 5;
      const int p3 = 1536 + (tid - 320);
      for (unsigned spins = 0; spins < SPIN_CAP; ++spins) {
#pragma unroll
        for (int i = 0; i < 3; ++i) a[i] = bld4<SC1>(rws, WS_G + tid * 16, i * 8192);
        bool ok = true;
        if (four) { a[3] = bld4<SC1>(rws, WS_G + p3 * 16, 0); ok = a[3][1] == tag && a[3][3] == tag; }
#pragma unroll
        for (int i = 0; i < 3; ++i) ok = ok && a[i][1] == tag && a[i][3] == tag;
        if (__all(ok)) break;
        asm volatile("" ::: "memory");
      }
#pragma unroll
      for (int i = 0; i < 4; ++i) {
        const int p = i < 3 ? tid + 512 * i : p3;
        if (i < 3 || four) *(u32x2*)(lds + L_ACT + ((p >> 2) & 3) * GROW + (p >> 4) * 32 + (p & 3) * 8) = u32x2{a[i][0], a[i][2]};
      }
    }
    STAMP(8);
    wg_barrier();
    {
      f32x4 a0 = {0, 0, 0, 0}, a1 = {0, 0, 0, 0};
#pragma unroll
      for (int n = 0; n < 7; ++n) {
        const u32x4 gb = *(const u32x4*)(lds + L_ACT + (c & 3) * GROW + (n * 8 + wave) * 64 + g * 16);
        if (n & 1) a1 = mfma(af[n], gb, a1); else a0 = mfma(af[n], gb, a0);
      }
      a0 += a1;
      if (c < 4) *(f32x4*)((float*)(lds + L_RED) + wave * 64 + c * 16 + 4 * g) = a0;
    }
    wg_barrier();
    if (wave == 0) {
      const float* rd = (const float*)(lds + L_RED) + lane;
      const float y = ((rd[0] + rd[64]) + (rd[128] + rd[192])) + ((rd[256] + rd[320]) + (rd[384] + rd[448]));
      const float r = hres + rbf(y);
      const float rn = dppf<0xB1>(r);
      if (!(lane & 1)) *(unsigned*)(out + g * D + 16 * ridx + c) = cvtpk(r, rn);
      if (ridx == 0 && lane == 0) __builtin_amdgcn_raw_buffer_store_b32(tag, rws, WS_EPOCH, 0, SC1);
      STAMP(9);
    }
  }
  STAMP_FLUSH;
}

extern "C" void kernel_launch(void* const* d_in, const int* in_sizes, int n_in, void* d_out, int out_size, void* d_ws, size_t ws_size,
                              hipStream_t stream) {
  fused_kernel<<<256, 512, 0, stream>>>((const u16*)d_in[0], (const u16*)d_in[1], (const u16*)d_in[2], (const u16*)d_in[3], (const u16*)d_in[4],
                                        (const u16*)d_in[6], (const u16*)d_in[7], (const u16*)d_in[8], (u16*)d_out, (unsigned char*)d_ws);
}
```

```cpp
#include <hip/hip_runtime.h>
#include <stdint.h>
#include <type_traits>

#define DSTEADY 5
#define DMAX 8
constexpr int T16 = 234, T16L = 222, KSW = 9;
constexpr int A0 = 70, BIAS = 10, FBOOST = 90;
constexpr int TH0 = 20, TH1 = 120, TH2 = 240, TL0 = 80, TL1 = 160;
constexpr int G1 = 9, G2 = 9;
constexpr int PPRE = 50, CUR = 3, XV = 3;
constexpr int RSTEP = 2;
constexpr int SLOT = 34, FB = 4, WBT = 300, NLATE = 8;
constexpr int QT = 350, QD = 60;
constexpr int NE = 15;
constexpr int DY = 192, DHH = 248, DF = 79, DZF = 74;
constexpr int FFS = 64;

typedef unsigned u32x4 __attribute__((ext_vector_type(4)));
typedef unsigned u32x2 __attribute__((ext_vector_type(2)));
typedef float f32x4 __attribute__((ext_vector_type(4)));
typedef float f32x2 __attribute__((ext_vector_type(2)));
typedef __bf16 bf16x8 __attribute__((ext_vector_type(8)));
typedef __bf16 bf16x2 __attribute__((ext_vector_type(2)));
typedef short s16x4 __attribute__((ext_vector_type(4)));
typedef unsigned short u16;
typedef unsigned long long u64;
#define LAS __attribute__((address_space(3)))

constexpr int D = 640, H = 32, SK = 4096, DH = 128, F = 1728, HD = H * DH;
constexpr float EPS = 1e-5f;

constexpr int WS_EPOCH = 0;
constexpr int WS_ML = 8192;
constexpr int WS_NEAR = 4096;
constexpr int WS_Q = 65536;
constexpr int WS_CTL = WS_Q + 128 * 4096;
constexpr int WS_BYTES = 10 << 20;
constexpr int OOB = 0x7fffff00;
static_assert(WS_CTL + 6 * 8192 <= (1 << 20), "ws");

constexpr int L_Q = 0;
constexpr int L_OP = 256;
constexpr int L_ST = 2304;
constexpr int L_GS = 2368;
constexpr int L_ACT = 2688;
constexpr int OROW = 256 + 16, HROW = 1280 + 32, GROW = 80;
constexpr int L_CTL = L_ACT + 1280;
constexpr int L_TAG = L_ACT + 1296;
constexpr int L_X = L_ACT + HROW + 1280;
constexpr int L_NEAR = L_ACT + 2 * HROW + 1280;
constexpr int L_WT = L_ACT + 4 * HROW;
constexpr int DROW = 1280 + 16;
constexpr int L_BYTES = L_WT + 81920 + 32 * DROW;
static_assert(L_WT % 16 == 0 && L_ACT % 16 == 0 && L_GS % 16 == 0, "align");

__device__ __forceinline__ float bf2f(unsigned v) { return __builtin_bit_cast(float, v << 16); }
__device__ __forceinline__ float bfhi(unsigned v) { return __builtin_bit_cast(float, v & 0xffff0000u); }
__device__ __forceinline__ float asf(unsigned v) { return __builtin_bit_cast(float, v); }
__device__ __forceinline__ unsigned asu(float v) { return __builtin_bit_cast(unsigned, v); }
__device__ __forceinline__ unsigned cvtpk(float lo, float hi) {
  bf16x2 r = {(__bf16)lo, (__bf16)hi};
  return __builtin_bit_cast(unsigned, r);
}
__device__ __forceinline__ float rbf(float x) { return (float)(__bf16)x; }
template <int CTRL> __device__ __forceinline__ float dppf(float x) {
  return __builtin_bit_cast(float, __builtin_amdgcn_mov_dpp(__builtin_bit_cast(int, x), CTRL, 0xf, 0xf, true));
}
template <int N> __device__ __forceinline__ unsigned bcast(unsigned x) { return __builtin_amdgcn_mov_dpp(x, 0x150 + N, 0xf, 0xf, true); }
__device__ __forceinline__ float row_sum(float x) {
  x += dppf<0x128>(x); x += dppf<0x124>(x); x += dppf<0x122>(x); x += dppf<0x121>(x); return x;
}
__device__ __forceinline__ float row_max(float x) {
  x = fmaxf(x, dppf<0x128>(x)); x = fmaxf(x, dppf<0x124>(x)); x = fmaxf(x, dppf<0x122>(x)); x = fmaxf(x, dppf<0x121>(x)); return x;
}
__device__ __forceinline__ float x16_sum(float x) {
  auto s = __builtin_amdgcn_permlane16_swap(asu(x), asu(x), false, false);
  return asf(s[0]) + asf(s[1]);
}
__device__ __forceinline__ float x32_sum(float x) {
  auto t = __builtin_amdgcn_permlane32_swap(asu(x), asu(x), false, false);
  return asf(t[0]) + asf(t[1]);
}
__device__ __forceinline__ float xrow_max(float x) {
  auto s = __builtin_amdgcn_permlane16_swap(asu(x), asu(x), false, false);
  x = fmaxf(asf(s[0]), asf(s[1]));
  auto t = __builtin_amdgcn_permlane32_swap(asu(x), asu(x), false, false);
  return fmaxf(asf(t[0]), asf(t[1]));
}
__device__ __forceinline__ float wave_sum(float x) { return x32_sum(x16_sum(row_sum(x))); }
__device__ __forceinline__ float wave_max(float x) { return xrow_max(row_max(x)); }
__device__ __forceinline__ float dot2(unsigned a, unsigned b, float c) {
  return __builtin_amdgcn_fdot2_f32_bf16(__builtin_bit_cast(bf16x2, a), __builtin_bit_cast(bf16x2, b), c, false);
}
__device__ __forceinline__ f32x4 mfma(u32x4 a, u32x4 b, f32x4 c) {
  return __builtin_amdgcn_mfma_f32_16x16x32_bf16(__builtin_bit_cast(bf16x8, a), __builtin_bit_cast(bf16x8, b), c, 0, 0, 0);
}
template <int I> __device__ __forceinline__ u32x4 selfrag(unsigned vdup, const u32x4& m) {
  unsigned t = bcast<I>(vdup);
  return u32x4{t & m[0], t & m[1], t & m[2], t & m[3]};
}
template <int I, int N, class Fn> __device__ __forceinline__ void static_for(Fn&& f) {
  if constexpr (I < N) { f(std::integral_constant<int, I>{}); static_for<I + 1, N>(f); }
}
typedef __amdgpu_buffer_rsrc_t rsrc_t;
__device__ __forceinline__ rsrc_t mkrs(const void* p, unsigned bytes) { return __builtin_amdgcn_make_buffer_rsrc((void*)p, 0, bytes, 0x00020000); }
template <int AUX> __device__ __forceinline__ u32x4 bld4(rsrc_t r, int voff, int soff) { return __builtin_amdgcn_raw_buffer_load_b128(r, voff, soff, AUX); }
template <int AUX> __device__ __forceinline__ u32x2 bld2(rsrc_t r, int voff, int soff) { return __builtin_amdgcn_raw_buffer_load_b64(r, voff, soff, AUX); }
constexpr int SC1 = 16, NT = 2;
__device__ __forceinline__ void st_gran1(rsrc_t r, int off, unsigned v, unsigned tag) { __builtin_amdgcn_raw_buffer_store_b64(u32x2{v, tag}, r, off, 0, SC1); }
__device__ __forceinline__ void st_gran2(rsrc_t r, int off, float a, float b, unsigned tag) {
  __builtin_amdgcn_raw_buffer_store_b128(u32x4{asu(a), tag, asu(b), tag}, r, off, 0, SC1);
}
__device__ __forceinline__ void st_sector(rsrc_t r, int off, bool on, unsigned v, unsigned tag) {
  __builtin_amdgcn_raw_buffer_store_b128(u32x4{v, tag, v, tag}, r, on ? off : OOB, 0, SC1);
}
__device__ __forceinline__ unsigned bad2(const u32x4& v, unsigned tag) { return (v[1] ^ tag) | (v[3] ^ tag); }
__device__ __forceinline__ void wg_barrier() {
  asm volatile("s_waitcnt lgkmcnt(0)" ::: "memory");
  __builtin_amdgcn_s_barrier();
  asm volatile("" ::: "memory");
}
constexpr unsigned SPIN_CAP = 1u << 20;
#define POLL(CAP, SLEEP, BODY, BAD)                                   \
  do {                                                                \
    unsigned n_ = (CAP);                                              \
    _Pragma("unroll 1") for (;;) {                                    \
      asm volatile("" ::: "memory");                                  \
      BODY;                                                           \
      if (__builtin_expect(__ballot((BAD) != 0) == 0, 1)) break;      \
      if (--n_ == 0) break;                                           \
      if (SLEEP) __builtin_amdgcn_s_sleep(SLEEP);                     \
    }                                                                 \
  } while (0)

__device__ __forceinline__ float gelu_tanh(float x) {
  const float u = x * __builtin_fmaf(x * x, 0.044715f * 2.302208198f, 2.302208198f);
  const float e = __builtin_amdgcn_exp2f(u);
  return x - x * __builtin_amdgcn_rcpf(1.f + e);
}
__device__ __forceinline__ u32x4 trfrag(const unsigned char* p, int second) {
  s16x4 lo = __builtin_amdgcn_ds_read_tr16_b64_v4i16((LAS s16x4*)p);
  s16x4 hi = __builtin_amdgcn_ds_read_tr16_b64_v4i16((LAS s16x4*)(p + second));
  u32x2 l2 = __builtin_bit_cast(u32x2, lo), h2 = __builtin_bit_cast(u32x2, hi);
  return u32x4{l2[0], l2[1], h2[0], h2[1]};
}
__device__ __forceinline__ unsigned rtime() { return (unsigned)__builtin_amdgcn_s_memrealtime(); }
__device__ __forceinline__ void wait_until(unsigned t) {
  unsigned now = rtime();
  if ((int)(t - now) > 2000) t = now;
#pragma unroll 1
  while ((int)(now - t) < 0) now = rtime();
}
__device__ __forceinline__ void wait_sleep(unsigned t) {
  unsigned now = rtime();
  if ((int)(t - now) > 2000) t = now;
#pragma unroll 1
  while ((int)(now - t) < 0) { __builtin_amdgcn_s_sleep(1); now = rtime(); }
}
constexpr unsigned long long gtab() { unsigned long long t = 0; for (int k = 0; k < 32; ++k) t |= (unsigned long long)(k < G1 ? 0 : k < G2 ? 1 : 2) << (2 * k); return t; }
constexpr unsigned GTAB = (unsigned)gtab();
constexpr int k_issued(int i) {
  int a = PPRE + i / CUR, b = i + DMAX;
  return a > b ? a : b;
}
#define STR2(x) #x
#define STR(x) STR2(x)
template <int J> __device__ __forceinline__ void throttle(int lag) {
  constexpr int th = (J & 1) ? TH0 : (J & 2) ? TH1 : TH2, tl = (J & 1) ? -TL0 : -TL1;
  asm volatile("s_waitcnt vmcnt(" STR(DMAX) "-1)\n\ts_cmpk_gt_i32 %0, %1\n\ts_cbranch_scc1 1f\n\ts_waitcnt vmcnt(" STR(DSTEADY) "-1)\n\t"
               "s_cmpk_gt_i32 %0, %2\n\ts_cbranch_scc1 1f\n\ts_waitcnt vmcnt(" STR(DSTEADY) "-2)\n1:" ::"s"(lag), "i"(th), "i"(tl) : "scc");
  __builtin_amdgcn_sched_barrier(0);
}

__global__ __launch_bounds__(512, 2) void fused_kernel(const u16* kc_, const u16* wq_, const u16* x_, unsigned char* ws_, const u16* vc_,
                                                       const u16* wo_, const u16* wg_, const u16* wu_, const u16* wd_, u16* out_) {
  __shared__ __attribute__((aligned(16))) unsigned char lds[L_BYTES];
  const int wave = __builtin_amdgcn_readfirstlane(threadIdx.x >> 6);
  asm volatile(".Lfk_beg:");
  typedef u64 u64x8 __attribute__((ext_vector_type(8)));
  typedef u64 u64x2 __attribute__((ext_vector_type(2)));
  u64x8 p0;
  u64x2 p1;
  asm volatile("s_load_dwordx16 %0, %2, 0x0\n\ts_load_dwordx4 %1, %2, 0x40" : "=&s"(p0), "=&s"(p1) : "s"(__builtin_amdgcn_kernarg_segment_ptr()));
  __builtin_amdgcn_sched_barrier(0);
  const int bid = blockIdx.x, lane = threadIdx.x & 63;
  const int xcd = bid & 7, slot = bid >> 3;
  const int h = xcd * 4 + (slot >> 3), b = (slot >> 1) & 3, half = slot & 1, jj = slot & 7;
  const int g = lane >> 4, c = lane & 15, e = lane & 7;
  auto code_prefetch = [&]() -> unsigned {
    u64 pc;
    unsigned d0, e0, e1;
    asm volatile("s_getpc_b64 %0\n1:\n\ts_mov_b32 %1, 1b-.Lfk_beg\n\ts_mov_b32 %2, .Lfk_e2-.Lfk_beg\n\ts_mov_b32 %3, .Lfk_e3-.Lfk_beg\n\ts_max_u32 %2, %2, %3"
                 : "=s"(pc), "=s"(d0), "=s"(e0), "=s"(e1) : : "scc");
    return __builtin_amdgcn_raw_buffer_load_b32(mkrs((const void*)(pc - d0), wave == 5 ? e0 : 0u), (lane * 16 + (slot & 15)) * 128, 0, 0);
  };
  const size_t kvoff = ((size_t)(b * H + h) * SK + half * 2048) * DH;
  const unsigned pf0 = code_prefetch();
  u32x4 r0[7];
  unsigned tag = 0;
  {
    const unsigned size = wave < 4 ? 4u * H * SK * DH * 2 : D * HD * 2;
    const int m1 = wave < 4 ? 128 : HD * 2, stride = wave < 4 ? 4096 : 8 * HD * 2;
    const int c0 = wave < 4 ? (int)kvoff * 2 + wave * 1024 : ((160 * b + (wave - 5) * 56) * HD + h * DH + 64 * half) * 2;
    const int vo = (lane >> 3) * m1 + e * 16 + c0;
    asm volatile("s_waitcnt lgkmcnt(0)" : "+s"(p0), "+s"(p1) : "s"(__builtin_amdgcn_kernarg_segment_ptr()));
    if (wave != 4) {
      const rsrc_t r = mkrs((const void*)(wave < 4 ? p0[0] : p0[1]), size);
#pragma unroll
      for (int i = 0; i < 7; ++i) r0[i] = bld4<NT>(r, vo + i * stride, 0);
    } else {
      const rsrc_t r = mkrs((const void*)p0[2], 4 * D * 2);
#pragma unroll
      for (int i = 0; i < 5; ++i) r0[i] = bld4<0>(r, (lane >> 3) * 640 + e * 16 + i * 128, 0);
      tag = __builtin_amdgcn_raw_buffer_load_b32(mkrs((const void*)p0[3], 64), WS_EPOCH, 0, SC1);
    }
  }
  __builtin_amdgcn_sched_barrier(0);
  const unsigned t00 = rtime();
  if (threadIdx.x < 2) *(u32x4*)(lds + L_CTL + threadIdx.x * 16) = u32x4{0u, 0u, 0u, 0u};
  wg_barrier();
  typedef const __attribute__((address_space(1))) u16* gu16;
  const u16 *kc = (const u16*)p0[0], *vc = (const u16*)p0[4], *wo = (const u16*)p0[5], *wg = (const u16*)p0[6], *wu = (const u16*)p0[7];
  const gu16 x = (gu16)p0[2];
  unsigned char* ws = (unsigned char*)p0[3];
  const rsrc_t rws = mkrs(ws, WS_BYTES);
  const int i3 = (slot >> 3) * 3 + jj - 5;
  const int xr = xcd == 0 ? 0 : xcd == 4 ? 1 : xcd == 2 ? 2 : 7;
  const int pr = (i3 >> 1) * 8 + (i3 >= 6 ? xr : xcd);
  const int role = jj < 5 ? 3 : (i3 < 8 && pr < 27) ? 1 : bid == 255 ? 2 : 0;
  const int ridx = pr * 2 + (i3 & 1);
  const int arena0 = (2 << 20) - (int)((u64)ws & 0xfffffu);
  unsigned nearm = 0;
  auto slotof = [&](int grp, int sl) { return arena0 + grp * (1 << 20) + sl * 16384 + (int)((nearm >> grp) & 1u) * 8192; };
  auto ysl = [&](int t) { const int gq = t / 20, r = t - gq * 20; return slotof(gq, 20 + (r >> 1)) + (r & 1) * 4096; };
  auto zsl = [&](int t) { const int gq = t / 20, r = t - gq * 20; return slotof(gq, r) + (r & 7) * 128; };
  auto osl = [&]() { return slotof(xcd, 32 + ((h >> 1) & 1)) + (h & 1) * 4096; };
  const f32x4 zero = {0, 0, 0, 0};
  auto wait_q = [&](auto SL) {
    u32x2 tf;
    POLL(SPIN_CAP * 16, SL.value, tf = *(volatile const LAS u32x2*)(lds + L_TAG), tf[1] == 0u);
    asm volatile("" ::: "memory");
    tag = tf[0];
  };
  auto wait_go = [&](auto SL) -> unsigned {
    u32x2 f;
    POLL(SPIN_CAP, SL.value, f = bld2<SC1>(rws, slotof(xcd, 35), 0), f[1] ^ tag);
    return __builtin_amdgcn_readfirstlane(f[0]);
  };
  auto stage_wd = [&]() {
    u32x4 wdl[5], wl[NLATE / 4 + 1];
    __builtin_amdgcn_s_sleep(4);
    const rsrc_t rD = mkrs((const void*)p1[0], F * D * 2), rL = mkrs((wave & 1) ? wu : wg, D * F * 2);
#pragma unroll
    for (int n = 0; n < NLATE / 4; ++n)
      wl[n] = bld4<NT>(rL, (lane >> 2) * (F * 2) + (lane & 3) * 16 + ridx * 64, (40 - NLATE + n * 4 + (wave >> 1)) * 16 * F * 2);
#pragma unroll
    for (int n = 0; n < 5; ++n) wdl[n] = bld4<NT>(rD, lane * 16, ridx * 40960 + (wave * 5 + n) * 1024);
#pragma unroll
    for (int n = 0; n < NLATE / 4; ++n) *(u32x4*)(lds + L_WT + (wave & 1) * 40960 + (40 - NLATE + n * 4 + (wave >> 1)) * 1024 + lane * 16) = wl[n];
#pragma unroll
    for (int n = 0; n < 5; ++n) {
      const int p = n * 64 + lane;
      *(u32x4*)(lds + L_WT + 81920 + (wave * 4 + p / 80) * DROW + (p % 80) * 16) = wdl[n];
    }
    wg_barrier();
  };
  auto coordinate = [&]() {
    u32x2 f;
    POLL(SPIN_CAP, 0, f = bld2<SC1>(rws, slotof(7, 36 + (wave >> 2)) + (wave & 3) * 2048 + (lane & 31) * 64, 0), f[1] ^ tag);
    const unsigned ref = rtime();
    const float dw = wave_max((float)(int)(f[0] - ref));
    if (lane == 0) *(unsigned*)(lds + L_ST + wave * 4) = ref + (unsigned)(int)dw;
    wg_barrier();
    if (wave == 0) {
      const unsigned tv = *(const unsigned*)(lds + L_ST + e * 4);
      float dm = (float)(int)(tv - ref);
      dm = fmaxf(dm, dppf<0x124>(dm)); dm = fmaxf(dm, dppf<0x122>(dm)); dm = fmaxf(dm, dppf<0x121>(dm));
      st_sector(rws, slotof((lane >> 2) & 7, 35) + (lane & 3) * 16, lane < 32, ref + (unsigned)(int)dm, tag);
    }
  };
  if (__builtin_expect(wave < 4, 1)) {
    const rsrc_t rk = mkrs(kc + kvoff, 512 * 1024);
    const int voff = wave * 1024 + lane * 16;
    constexpr int DM = DMAX;
    u32x4 k0[64 + DM];
#pragma unroll
    for (int j = 0; j < 7; ++j) k0[j] = r0[j];
    const rsrc_t rv = mkrs(vc + kvoff, 512 * 1024);
    unsigned sref = t00 + A0;
    unsigned koff = T16, kcnt = 1;
    int lag = 1000;
    const int boost = role == 1 ? FBOOST : role == 2 ? 60 : 0;
    unsigned tq = 0, rq_ = 0;
    auto request = [&](bool rel) {
      tq = rtime();
      if (rel) rq_ = *(volatile const LAS unsigned*)(lds + L_CTL + 8);
      __builtin_amdgcn_sched_barrier(0);
    };
    auto update = [&](bool rel) {
      const unsigned sv = tq - koff;
      if (rel) sref = __builtin_amdgcn_readfirstlane(rq_);
      lag = ((int)(sv - sref) + boost) << ((GTAB >> (2 * kcnt)) & 3u);
      if (!rel) lag = max(lag, 0);
      if (wave == 0) *(volatile LAS u32x2*)(lds + L_CTL) = u32x2{sv, kcnt};
      koff += kcnt < KSW ? T16 : T16L;
      ++kcnt;
    };
#define POST(J, REL) do { __builtin_amdgcn_sched_barrier(0); if constexpr ((J) % 16 == 15) request(REL); } while (0)
    u32x4 msk;
#pragma unroll
    for (int r = 0; r < 4; ++r) msk[r] = (c < 8 && (c >> 1) == r) ? ((c & 1) ? 0xffff0000u : 0x0000ffffu) : 0u;
    static_for<7, PPRE>([&](auto J) {
      constexpr int j = J.value;
      if constexpr (j % 16 == 0) update(false);
      throttle<j>(lag);
      k0[j] = bld4<NT>(rk, voff, j * 4096);
      POST(j, j + 1 >= PPRE);
    });
    static_assert(PPRE % 16 != 0, "the request for the first relative update must follow the q wait");
    wait_q(std::integral_constant<int, 1>{});
    const u32x4 q = *(const u32x4*)(lds + L_Q + 16 * c);
    float sc[8];
    auto kstep = [&](u32x4 k) -> float {
      float d = dot2(k[0], q[0], 0.f);
      d = dot2(k[1], q[1], d); d = dot2(k[2], q[2], d); d = dot2(k[3], q[3], d);
      return row_sum(d);
    };
    {
      float kk[4] = {0.f, 0.f, 0.f, 0.f};
      static_for<0, 64>([&](auto I) {
        constexpr int i = I.value;
        constexpr int lo = k_issued(i), hi = k_issued(i + 1);
        float d = kstep(k0[i]);
        kk[i >> 4] = (c == (i & 15)) ? d : kk[i >> 4];
        if constexpr (hi > lo) {
          asm volatile("" : "+v"(kk[0]), "+v"(kk[1]), "+v"(kk[2]), "+v"(kk[3]));
          static_for<lo, hi>([&](auto J) {
            constexpr int j = J.value;
            if constexpr (j % 16 == 0) update(true);
            throttle<j>(lag);
            k0[j] = bld4<NT>(rk, voff, j * 4096);
            POST(j, true);
          });
        }
      });
      static_assert(k_issued(64) == 64 + DM && k_issued(0) == PPRE, "catch-up must finish inside the peeled part");
      sc[4] = kk[0]; sc[5] = kk[1]; sc[6] = kk[2]; sc[7] = kk[3];
    }
    u32x4 ring[32];
#pragma unroll
    for (int j = 0; j < DM; ++j) ring[j] = k0[64 + j];
#pragma unroll 1
    for (int it = 2; it < 4; ++it) {
#pragma unroll
      for (int i = 0; i < 6; ++i) sc[i] = sc[i + 2];
      const rsrc_t rn = it == 3 ? rv : rk;
      const int so = it * 32 * 4096, son = it == 3 ? 0 : so + 32 * 4096;
      float ka = 0.f, kb = 0.f;
      static_for<0, 32>([&](auto I) {
        constexpr int i = I.value;
        if constexpr ((i + DM) % 16 == 0) update(true);
        throttle<i + DM>(lag);
        u32x4 cur = ring[i];
        if constexpr (i + DM < 32) ring[i + DM] = bld4<NT>(rk, voff, so + (i + DM) * 4096);
        else ring[i + DM - 32] = bld4<NT>(rn, voff, son + (i + DM - 32) * 4096);
        POST(i + DM, true);
        float d = kstep(cur);
        if constexpr (i < 16) { ka = (c == i) ? d : ka; asm volatile("" : "+v"(ka)); } else { kb = (c == i - 16) ? d : kb; asm volatile("" : "+v"(kb)); }
        __builtin_amdgcn_sched_barrier(0);
      });
      sc[6] = ka; sc[7] = kb;
    }
    constexpr int DV = DM + XV;
    static_assert(DV < 15, "update points");
#pragma unroll
    for (int j = DM; j < DV; ++j) ring[j] = bld4<NT>(rv, voff, j * 4096);
    __builtin_amdgcn_sched_barrier(0);
    constexpr float SCALE = 0.08838834764831845f, LOG2E = 1.4426950408889634f;
    float m = -INFINITY;
#pragma unroll
    for (int i = 0; i < 8; ++i) { sc[i] = rbf(rbf(sc[i]) * SCALE) * LOG2E; m = fmaxf(m, sc[i]); }
    m = wave_max(m);
    unsigned pd[8];
    float l = 0.f;
#pragma unroll
    for (int i = 0; i < 8; ++i) { float p = __builtin_amdgcn_exp2f(sc[i] - m); pd[i] = cvtpk(p, p); l += bfhi(pd[i]); }
    l = wave_sum(l);
    if (lane == 0) {
      *(volatile LAS f32x2*)(lds + L_ST + wave * 8) = f32x2{m, l};
      asm volatile("s_waitcnt lgkmcnt(0)" ::: "memory");
      __hip_atomic_fetch_add((LAS unsigned*)(lds + L_TAG + 12), 1u, __ATOMIC_RELAXED, __HIP_MEMORY_SCOPE_WORKGROUP);
    }
    f32x4 acc0 = {0, 0, 0, 0}, acc1 = {0, 0, 0, 0};
#pragma unroll 1
    for (int it = 0; it < 3; ++it) {
      const int so = it * 32 * 4096;
      static_for<0, 32>([&](auto I) {
        constexpr int i = I.value;
        if constexpr ((i + DV) % 16 == 0) update(true);
        throttle<i + DV>(lag);
        u32x4 cur = ring[i];
        ring[(i + DV) % 32] = bld4<NT>(rv, voff, so + (i + DV) * 4096);
        POST(i + DV, true);
        u32x4 a = selfrag<i & 15>(pd[i >> 4], msk);
        if constexpr (i & 1) { acc1 = mfma(a, cur, acc1); asm volatile("" : "+v"(acc1)); } else { acc0 = mfma(a, cur, acc0); asm volatile("" : "+v"(acc0)); }
        __builtin_amdgcn_sched_barrier(0);
      });
#pragma unroll
      for (int i = 0; i < 6; ++i) pd[i] = pd[i + 2];
    }
    static_for<0, 32>([&](auto I) {
      constexpr int i = I.value;
      u32x4 cur = ring[i];
      if constexpr (i + DV < 32) {
        if constexpr ((i + DV) % 16 == 0) update(true);
        if constexpr (i + DV == 24) lag = ((int)(tq - (koff - T16L / 2) - sref) + boost) * 4;
        throttle<i + DV>(lag);
        ring[i + DV] = bld4<NT>(rv, voff, (96 + i + DV) * 4096);
        if constexpr (i + DV < 31) POST(i + DV, true);
        if constexpr (i + DV == 23) request(false);
      }
      __builtin_amdgcn_sched_barrier(0);
      u32x4 a = selfrag<i & 15>(pd[i >> 4], msk);
      if constexpr (i & 1) { acc1 = mfma(a, cur, acc1); asm volatile("" : "+v"(acc1)); } else { acc0 = mfma(a, cur, acc0); asm volatile("" : "+v"(acc0)); }
      if constexpr (i == 31 - NE)
        if (lane == 0) __hip_atomic_fetch_add((LAS unsigned*)(lds + L_TAG + 12), 16u, __ATOMIC_RELAXED, __HIP_MEMORY_SCOPE_WORKGROUP);
      __builtin_amdgcn_sched_barrier(0);
    });
    acc0 += acc1;
    if (lane < 32) *(f32x4*)(lds + L_OP + (wave * 128 + 8 * c + 4 * g) * 4) = acc0;
    wg_barrier();
    nearm = __builtin_amdgcn_readfirstlane(*(const unsigned*)(lds + L_NEAR));
    if (role == 3) {
        const int task = h * 5 + jj, rb = task / 40, cs = task % 40;
        const int col = rb * D + 16 * cs + e;
        int go = osl() + wave * 1024 + lane * 16, lw = L_ACT + wave * OROW + (lane & 31) * 8 + (lane >> 5) * 4, gof = slotof(xcd, 35);
        int yo = ysl(task) + lane * 16, zo = zsl(task) + lane * 16, zo6 = lane < 48 ? zo : zo - 6 * 1024;
        int ho = slotof(lane >> 3, 30 + task / 80) + (task % 80) * 64 + e * 8, oo = lane < 16 ? (col + (lane & 8)) * 2 : OOB;
        int fo = slotof(xcd, 34) + (lane < 54 ? lane : 0) * FFS;
        float zm = lane < 48 ? 1.f : 0.f;
        const unsigned xv = *(const unsigned*)(lds + L_X + e * 4);
        float xaf = bf2f(xv & 0xffff), xbf = bfhi(xv);
        asm volatile("" : "+v"(go), "+v"(lw), "+v"(gof), "+v"(yo), "+v"(zo), "+v"(zo6), "+v"(ho), "+v"(oo), "+v"(fo), "+v"(zm), "+v"(xaf), "+v"(xbf));
        u32x4 a0;
        u32x2 gf;
        POLL(SPIN_CAP, 0, { a0 = bld4<SC1>(rws, go, 0); gf = bld2<SC1>(rws, gof, 0); }, bad2(a0, tag));
        auto s0 = __builtin_amdgcn_permlane32_swap(a0[0], a0[2], false, false);
        *(unsigned*)(lds + lw) = cvtpk(bf2f(s0[0] & 0xffff) + bf2f(s0[1] & 0xffff), bfhi(s0[0]) + bfhi(s0[1]));
        if (lane == 0) *(u32x2*)(lds + L_ST + wave * 8) = gf;
        wg_barrier();
        if (wave == 0) {
          const rsrc_t ro = mkrs((const void*)p1[1], 4 * D * 2);
          gf = *(const u32x2*)(lds + L_ST + (lane & 3) * 8);
          const u64 gm = __ballot(gf[1] == tag);
          unsigned tA = __builtin_amdgcn_readlane(gf[0], __builtin_ctzll(gm | 8u));
          if (!gm) tA = wait_go(std::integral_constant<int, 1>{});
          wait_until(tA + DY);
          u32x4 y[7];
          POLL(SPIN_CAP, 0, { _Pragma("unroll") for (int i = 0; i < 4; ++i) y[i] = bld4<SC1>(rws, yo, i * 1024); },
               bad2(y[0], tag) | bad2(y[1], tag) | bad2(y[2], tag) | bad2(y[3], tag));
          float s0 = (asf(y[0][0]) + asf(y[1][0])) + (asf(y[2][0]) + asf(y[3][0]));
          float s1 = (asf(y[0][2]) + asf(y[1][2])) + (asf(y[2][2]) + asf(y[3][2]));
          s0 += dppf<0x128>(s0); s1 += dppf<0x128>(s1);
          s0 = x16_sum(s0); s1 = x16_sum(s1);
          s0 = x32_sum(s0); s1 = x32_sum(s1);
          const unsigned sr = cvtpk(s0, s1);
          const unsigned hh = cvtpk(xaf + bf2f(sr & 0xffff), xbf + bfhi(sr));
          st_gran1(rws, ho, hh, tag);
          if (task == 0) st_sector(rws, WS_EPOCH + lane * 16, lane < 4, tag, tag);
          __builtin_amdgcn_raw_buffer_store_b16((u16)((lane & 8) ? hh >> 16 : hh), ro, oo, 0, 0);
          {
            wait_until(tA + DHH + DF);
            u32x2 ff;
            POLL(SPIN_CAP, 1, ff = bld2<SC1>(rws, fo, 0), ff[1] ^ tag);
            const float dm = wave_max((float)(int)(ff[0] - tA));
            wait_until(tA + (unsigned)__builtin_amdgcn_readfirstlane((int)dm) + DZF);
          }
          POLL(SPIN_CAP, 0, { _Pragma("unroll") for (int i = 0; i < 7; ++i) y[i] = bld4<SC1>(rws, i < 6 ? zo : zo6, i * 1024); },
               bad2(y[0], tag) | bad2(y[1], tag) | bad2(y[2], tag) | bad2(y[3], tag) | bad2(y[4], tag) | bad2(y[5], tag) | bad2(y[6], tag));
          float z0 = ((asf(y[0][0]) + asf(y[1][0])) + (asf(y[2][0]) + asf(y[3][0]))) + __builtin_fmaf(zm, asf(y[6][0]), asf(y[4][0]) + asf(y[5][0]));
          float z1 = ((asf(y[0][2]) + asf(y[1][2])) + (asf(y[2][2]) + asf(y[3][2]))) + __builtin_fmaf(zm, asf(y[6][2]), asf(y[4][2]) + asf(y[5][2]));
          z0 += dppf<0x128>(z0); z1 += dppf<0x128>(z1);
          z0 = x16_sum(z0); z1 = x16_sum(z1);
          z0 = x32_sum(z0); z1 = x32_sum(z1);
          const unsigned zr = cvtpk(z0, z1);
          const unsigned o2 = cvtpk(bf2f(hh & 0xffff) + bf2f(zr & 0xffff), bfhi(hh) + bfhi(zr));
          __builtin_amdgcn_raw_buffer_store_b16((u16)((lane & 8) ? o2 >> 16 : o2), ro, oo, 0, 0);
        }
    } else if (role == 1) {
      const int tp0 = 3 * wave;
      const unsigned char* dl = lds + L_WT + 81920 + (8 * g + (c >> 2)) * DROW + tp0 * 64 + (c & 1) * 8 + (c & 2) * 16;
      const int zt = g * 40 + tp0 * 2 + (c >> 3), zl = ridx * 128 + e * 16;
      int zoff = zsl(zt) + zl, zoff2 = zsl(zt + 2) + zl, zoff4 = zsl(zt + 4) + zl, lg = L_GS + (c >> 2) * GROW + g * 16;
        u32x4 ad[6];
        stage_wd();
#pragma unroll
        for (int u = 0; u < 6; ++u) ad[u] = trfrag(dl + (u >> 1) * 64 + (u & 1) * 16, 4 * DROW);
        int go = slotof(xcd, 30 + (wave >> 1)) + (wave & 1) * 2560 + lane * 16, go2 = lane < 32 ? go + 2048 : go;
        int l0 = L_ACT + wave * HROW + lane * 8, l2 = lane < 32 ? l0 + 1024 : l0;
        unsigned m2 = lane < 32 ? ~0u : 0u;
        int ffo = (wave == 0 && lane < 32) ? slotof((lane >> 2) & 7, 34) + ridx * FFS + (lane & 3) * 16 : OOB;
        asm volatile("" : "+v"(go), "+v"(go2), "+v"(l0), "+v"(l2), "+v"(m2), "+v"(zoff), "+v"(lg), "+v"(ffo), "+v"(zoff2), "+v"(zoff4));
#pragma unroll
        for (int u = 0; u < 6; ++u) asm volatile("" : "+v"(ad[u]));
        wait_until(wait_go(std::integral_constant<int, 4>{}) + DHH);
        u32x4 a0, a1, a2;
        POLL(SPIN_CAP, 0, { a0 = bld4<SC1>(rws, go, 0); a1 = bld4<SC1>(rws, go, 1024); a2 = bld4<SC1>(rws, go2, 0); },
             bad2(a0, tag) | bad2(a1, tag) | bad2(a2, tag));
        *(u32x2*)(lds + l0) = u32x2{a0[0], a0[2]};
        *(u32x2*)(lds + l0 + 512) = u32x2{a1[0], a1[2]};
        *(u32x2*)(lds + l2) = u32x2{a2[0], a2[2]};
        wg_barrier();
        st_sector(rws, ffo, true, rtime(), tag);
        const unsigned one2 = 0x3f803f80u, b0 = a2[0] & m2, b2 = a2[2] & m2;
        float s1 = dot2(a0[0], one2, 0.f), s2 = dot2(a0[0], a0[0], 0.f);
        s1 = dot2(a0[2], one2, s1); s2 = dot2(a0[2], a0[2], s2);
        s1 = dot2(a1[0], one2, s1); s2 = dot2(a1[0], a1[0], s2);
        s1 = dot2(a1[2], one2, s1); s2 = dot2(a1[2], a1[2], s2);
        s1 = dot2(b0, one2, s1); s2 = dot2(b0, b0, s2);
        s1 = dot2(b2, one2, s1); s2 = dot2(b2, b2, s2);
        s1 += dppf<0x128>(s1); s2 += dppf<0x128>(s2); s1 += dppf<0x124>(s1); s2 += dppf<0x124>(s2);
        s1 += dppf<0x122>(s1); s2 += dppf<0x122>(s2); s1 += dppf<0x121>(s1); s2 += dppf<0x121>(s2);
        s1 = x16_sum(s1); s2 = x16_sum(s2); s1 = x32_sum(s1); s2 = x32_sum(s2);
        const float mean = s1 * (1.f / D);
        const float var = fmaxf(s2 * (1.f / D) - mean * mean, 0.f);
        const float mb = rbf(mean), rs = __builtin_amdgcn_rcpf(rbf(__builtin_amdgcn_sqrtf(rbf(rbf(var) + EPS))));
        if (lane == 0) *(f32x2*)(lds + L_ST + wave * 8) = f32x2{mb, rs};
        wg_barrier();
        wg_barrier();
        const u32x4 gb = *(const u32x4*)(lds + lg);
        const f32x4 z0 = mfma(gb, ad[0], zero), z1 = mfma(gb, ad[1], zero), z2 = mfma(gb, ad[2], zero), z3 = mfma(gb, ad[3], zero);
        const f32x4 z4 = mfma(gb, ad[4], zero), z5 = mfma(gb, ad[5], zero);
        st_gran2(rws, zoff, z0[0], z1[0], tag);
        st_gran2(rws, zoff2, z2[0], z3[0], tag);
        st_gran2(rws, zoff4, z4[0], z5[0], tag);
    } else if (role == 2) coordinate();
  } else {
  float scl[4] = {0.f, 0.f, 0.f, 0.f};
  if (wave == 4) {
    const int kq = jj >> 1, ch = jj & 1, i2 = slot >> 1;
    asm volatile("v_add_u32 %0, 1, %0" : "+v"(tag));
    float s1 = 0.f, s2 = 0.f;
#pragma unroll
    for (int t = 0; t < 5; ++t) {
      *(u32x4*)(lds + L_ACT + g * HROW + (c >> 3) * 640 + t * 128 + e * 16) = r0[t];
#pragma unroll
      for (int r = 0; r < 4; ++r) s1 = dot2(r0[t][r], 0x3f803f80u, s1);
    }
    const float mean = row_sum(s1) * (1.f / D);
#pragma unroll
    for (int t = 0; t < 5; ++t)
#pragma unroll
      for (int r = 0; r < 4; ++r) { const float a = bf2f(r0[t][r] & 0xffff) - mean, bb = bfhi(r0[t][r]) - mean; s2 = __builtin_fmaf(a, a, s2); s2 = __builtin_fmaf(bb, bb, s2); }
    const float var = row_sum(s2) * (1.f / D);
    const float mb = rbf(mean), rs = 1.f / rbf(sqrtf(rbf(rbf(var) + EPS)));
    {
      asm volatile("" ::: "memory");
      unsigned* xl = (unsigned*)(lds + L_ACT + g * HROW + kq * 320 + c * 20);
#pragma unroll
      for (int t = 0; t < 5; ++t) {
        const unsigned v = xl[t];
        const unsigned d = cvtpk(bf2f(v & 0xffff) - mb, bfhi(v) - mb);
        xl[t] = cvtpk(bf2f(d & 0xffff) * rs, bfhi(d) * rs);
      }
    }
    {
      unsigned cnt;
      POLL(SPIN_CAP * 16, 1, cnt = *(volatile const LAS unsigned*)(lds + L_TAG + 8), cnt < 3u);
      asm volatile("" ::: "memory");
    }
    f32x4 qa[4];
#pragma unroll
    for (int u = 0; u < 4; ++u) qa[u] = f32x4{0, 0, 0, 0};
#pragma unroll
    for (int t = 0; t < 5; ++t) {
      const u32x4 xb = *(const u32x4*)(lds + L_ACT + (c >> 2) * HROW + (kq * 5 + t) * 64 + g * 16);
#pragma unroll
      for (int u = 0; u < 4; ++u)
        qa[u] = mfma(xb, trfrag(lds + L_WT + 81920 + (32 * t + 8 * g + (c >> 2)) * 128 + u * 32 + (c & 3) * 8, 512), qa[u]);
    }
    {
      const int off = WS_Q + (h * 4 + g) * 4096 + kq * 1024 + ch * 512 + c * 16;
      st_gran2(rws, off, qa[0][0], qa[1][0], tag);
      st_gran2(rws, off + 256, qa[2][0], qa[3][0], tag);
    }
    u32x4 qv[4];
    wait_sleep(rtime() + QD);
    wait_sleep(t00 + QT);
    POLL(SPIN_CAP, 8, {
      _Pragma("unroll") for (int i = 0; i < 4; ++i) qv[i] = bld4<SC1>(rws, WS_Q + (h * 4 + b) * 4096 + lane * 16, i * 1024);
    }, bad2(qv[0], tag) | bad2(qv[1], tag) | bad2(qv[2], tag) | bad2(qv[3], tag));
    {
      const unsigned qq = cvtpk((asf(qv[0][0]) + asf(qv[1][0])) + (asf(qv[2][0]) + asf(qv[3][0])), (asf(qv[0][2]) + asf(qv[1][2])) + (asf(qv[2][2]) + asf(qv[3][2])));
      *(u16*)(lds + L_Q + (32 * g + c) * 2) = (u16)qq;
      *(u16*)(lds + L_Q + (32 * g + 16 + c) * 2) = (u16)(qq >> 16);
    }
    if (lane == 0) *(unsigned*)(lds + L_CTL + 8) = t00 + A0;
    asm volatile("s_waitcnt lgkmcnt(0)" ::: "memory");
    if (lane == 0) *(volatile LAS u32x2*)(lds + L_TAG) = u32x2{tag, 1u};
    unsigned sprev = t00 + A0;
#pragma unroll 1
    for (int r = 0; r < (10 + RSTEP) / RSTEP; ++r) {
      u32x2 me;
#pragma unroll 1
      for (int sp = 0; sp < 4096; ++sp) {
        me = *(volatile const LAS u32x2*)(lds + L_CTL);
        if (__builtin_amdgcn_readfirstlane(me[1]) >= (unsigned)(RSTEP * r + 3)) break;
        __builtin_amdgcn_s_sleep(8);
      }
      st_sector(rws, WS_CTL + (r * 16 + i2) * 512 + xcd * 64 + lane * 16, !half && jj < 5 && lane < 4, me[0], tag);
      __builtin_amdgcn_s_sleep(72);
      const int la = (i2 & 12) + (i2 & 3) % 3, lb = ((i2 ^ 8) & 12) + ((i2 & 3) + 1) % 3;
      const u32x2 o = bld2<SC1>(rws, WS_CTL + (r * 16 + ((lane & 8) ? lb : la)) * 512 + e * 64, 0);
      const bool ok = o[1] == tag;
      const float n = row_sum(ok ? 1.f : 0.f), d = row_sum(ok ? (float)(int)(o[0] - me[0]) : 0.f);
      if (n >= 6.f) {
        const unsigned sn = me[0] + (unsigned)(int)(d / n);
        const int drift = r > 0 ? min(max((int)(sn - sprev), -20), 60) : 0;
        sprev = sn;
        if (lane == 0) *(volatile LAS unsigned*)(lds + L_CTL + 8) = sn + BIAS + drift * 2 / 4;
      }
    }
    u32x4 gm;
    POLL(SPIN_CAP, 64, gm = bld4<SC1>(rws, WS_ML + (b * H + h) * 128 + e * 16, 0), bad2(gm, tag));
    const float mw = asf(gm[0]), lw = asf(gm[2]);
    float M = fmaxf(mw, dppf<0x124>(mw)); M = fmaxf(M, dppf<0x122>(M)); M = fmaxf(M, dppf<0x121>(M));
    const float ew = __builtin_amdgcn_exp2f(mw - M);
    float L = ew * lw; L += dppf<0x124>(L); L += dppf<0x122>(L); L += dppf<0x121>(L);
    const float sv = ew / L;
#pragma unroll
    for (int s2 = 0; s2 < 4; ++s2) scl[s2] = asf(__builtin_amdgcn_readlane(asu(sv), half * 4 + s2));
  } else {
#pragma unroll
    for (int i = 0; i < 7; ++i) *(u32x4*)(lds + L_WT + 81920 + ((wave - 5) * 7 + i) * 1024 + lane * 16) = r0[i];
    asm volatile("s_waitcnt lgkmcnt(0)" ::: "memory");
    if (lane == 0) __hip_atomic_fetch_add((LAS unsigned*)(lds + L_TAG + 8), 1u, __ATOMIC_RELAXED, __HIP_MEMORY_SCOPE_WORKGROUP);
    asm volatile("" ::"v"(pf0));
    if (wave == 7 && role == 3) {
      const int task = h * 5 + jj, rb = task / 40, cs = task % 40;
      const int col = rb * D + 16 * cs + e;
      if (lane < 8) *(unsigned*)(lds + L_X + lane * 4) = (unsigned)x[col] | ((unsigned)x[col + 8] << 16);
    }
    wait_q(std::integral_constant<int, 16>{});
    if (role == 1) {
      const rsrc_t rG = mkrs(wg, D * F * 2), rU = mkrs(wu, D * F * 2);
      const int vo = (lane >> 2) * (F * 2) + (lane & 3) * 16 + ridx * 64;
      constexpr int NSL = (40 - NLATE + 3 * FB - 1) / (3 * FB);
      unsigned sl = rtime() / (3 * SLOT * FB) + 1;
#pragma unroll 1
      for (int i = 0; i < NSL; ++i, ++sl) {
        const int n0 = ((int)(sl % (unsigned)NSL) * 3 + wave - 5) * FB;
        wait_sleep(sl * 3 * SLOT * FB);
        u32x4 a[FB], u[FB];
#pragma unroll
        for (int k = 0; k < FB; ++k) { const int v2 = n0 + k < 40 - NLATE ? vo : OOB; a[k] = bld4<NT>(rG, v2, (n0 + k) * 16 * F * 2); u[k] = bld4<NT>(rU, v2, (n0 + k) * 16 * F * 2); }
#pragma unroll
        for (int k = 0; k < FB; ++k)
          if (n0 + k < 40 - NLATE) {
            *(u32x4*)(lds + L_WT + (n0 + k) * 1024 + lane * 16) = a[k];
            *(u32x4*)(lds + L_WT + 40960 + (n0 + k) * 1024 + lane * 16) = u[k];
          }
      }
    } else if (slot == 30 && wave == 7) {
      auto poff = [&](int i) { return arena0 + xcd * (1 << 20) + (40 + (i >> 1)) * 16384 + (i & 1) * 8192 + (i & 4) * 1024 + (i & 2) * 640; };
      st_sector(rws, poff(lane >> 2) + (lane & 3) * 16, true, 0u, 0u);
      asm volatile("s_waitcnt vmcnt(0)" ::: "memory");
      unsigned m0 = ~0u, m1 = ~0u, s0 = 0, s1 = 0;
#pragma unroll 1
      for (int i = 0; i < 16; ++i) {
        u64 c0, c1;
        unsigned v;
        asm volatile("s_memtime %0\n\ts_waitcnt lgkmcnt(0)\n\ts_load_dword %2, %3, %4\n\ts_waitcnt lgkmcnt(0)\n\ts_memtime %1\n\ts_waitcnt lgkmcnt(0)"
                     : "=&s"(c0), "=&s"(c1), "=&s"(v) : "s"(ws), "s"(poff(i)) : "memory");
        const unsigned dt = (unsigned)c1 - (unsigned)c0;
        if (i >= 2) { if (i & 1) { m1 = min(m1, dt); s1 += dt; } else { m0 = min(m0, dt); s0 += dt; } }
      }
      const unsigned pick = s1 + 7 * m1 < s0 + 7 * m0 ? 1u : 0u;
      st_sector(rws, WS_NEAR + xcd * 64 + lane * 16, lane < 4, pick, tag);
    } else if (role == 3) {
      const rsrc_t rW = mkrs(wo, HD * D * 2);
      wait_sleep((rtime() / WBT + 1) * WBT);
      u32x4 t[11];
#pragma unroll
      for (int k = 0; k < 11; ++k) {
        const int n = k * 3 + wave - 5;
        t[k] = bld4<NT>(rW, n < 32 ? g * (D * 2) + c * 16 : OOB, (h * DH + (n & 31) * 4) * (D * 2) + jj * 256);
      }
#pragma unroll
      for (int k = 0; k < 11; ++k)
        if (k * 3 + wave - 5 < 32) *(u32x4*)(lds + L_WT + (k * 3 + wave - 5) * 1024 + lane * 16) = t[k];
    }
    asm volatile("s_waitcnt lgkmcnt(0)" ::: "memory");
    if (lane == 0) __hip_atomic_fetch_add((LAS unsigned*)(lds + L_CTL + 12), 1u, __ATOMIC_RELAXED, __HIP_MEMORY_SCOPE_WORKGROUP);
  }
  if (wave == 5) {
    unsigned kc_;
    POLL(4096, 16, kc_ = *(volatile const LAS unsigned*)(lds + L_CTL + 4), kc_ < 8u);
    u32x4 nb;
    POLL(SPIN_CAP, 16, nb = bld4<SC1>(rws, WS_NEAR + e * 64, 0), bad2(nb, tag));
    const unsigned m = (unsigned)__ballot(nb[0] != 0u) & 0xffu;
    if (lane == 0) {
      *(volatile LAS unsigned*)(lds + L_NEAR) = m;
      asm volatile("s_waitcnt lgkmcnt(0)" ::: "memory");
      __hip_atomic_fetch_add((LAS unsigned*)(lds + L_CTL + 12), 1u, __ATOMIC_RELAXED, __HIP_MEMORY_SCOPE_WORKGROUP);
    }
  }
  {
    unsigned cnt;
    POLL(SPIN_CAP, 8, cnt = *(volatile const LAS unsigned*)(lds + L_CTL + 12), cnt < 4u);
    asm volatile("" ::: "memory");
    nearm = __builtin_amdgcn_readfirstlane(*(volatile const LAS unsigned*)(lds + L_NEAR));
  }
  auto bar2_publish = [&]() {
    if (wave > 4) {
      unsigned kc_;
      if (wave == 6) {
        POLL(SPIN_CAP, 16, kc_ = *(volatile const LAS unsigned*)(lds + L_TAG + 12), (kc_ & 15u) < 4u);
        const f32x2 ml = *(volatile const LAS f32x2*)(lds + L_ST + (lane & 3) * 8);
        st_gran2(rws, lane < 4 ? WS_ML + (b * H + h) * 128 + half * 64 + lane * 16 : OOB, ml[0], ml[1], tag);
      }
      POLL(4096, 16, kc_ = *(volatile const LAS unsigned*)(lds + L_CTL + 4), kc_ < 12u);
      const unsigned t = code_prefetch();
      asm volatile("" ::"v"(t));
      if (wave == 5) {
        POLL(SPIN_CAP, 1, kc_ = *(volatile const LAS unsigned*)(lds + L_TAG + 12), kc_ < 64u);
        st_sector(rws, slotof(7, 36 + (bid >> 7)) + (bid & 127) * 64 + lane * 16, lane < 4, rtime(), tag);
      }
    }
    int ooff = osl() + jj * 512 + lane * 8, lo = L_OP + lane * 8;
    asm volatile("" : "+v"(ooff), "+v"(lo));
    wg_barrier();
    if (wave == 4) {
      const f32x2 o0 = *(const f32x2*)(lds + lo), o1 = *(const f32x2*)(lds + lo + 512), o2 = *(const f32x2*)(lds + lo + 1024), o3 = *(const f32x2*)(lds + lo + 1536);
      const f32x2 o = (o0 * scl[0] + o1 * scl[1]) + (o2 * scl[2] + o3 * scl[3]);
      st_gran1(rws, ooff, cvtpk(o[0], o[1]), tag);
    }
  };
    if (role == 3) {
        u32x4 af[8];
        int yoff = ysl(g * 40 + (jj * 4 + wave - 4) * 2 + (c >> 3)) + h * 128 + e * 16, la = L_ACT + (c >> 2) * OROW + g * 16;
        const unsigned char* wl = lds + L_WT + (wave - 4) * 64 + (8 * g + (c >> 2)) * 256 + (c & 1) * 8 + (c & 2) * 16;
#pragma unroll
        for (int t = 0; t < 4; ++t) { af[2 * t] = trfrag(wl + t * 8192, 1024); af[2 * t + 1] = trfrag(wl + t * 8192 + 16, 1024); }
#pragma unroll
        for (int t = 0; t < 8; ++t) asm volatile("" : "+v"(af[t]));
        asm volatile("" : "+v"(yoff), "+v"(la));
        if (wave == 4) {
          u32x4 t[4];
#pragma unroll
          for (int i = 0; i < 4; ++i) t[i] = bld4<SC1>(rws, osl() + lane * 16, i * 1024);
#pragma unroll
          for (int i = 0; i < 4; ++i) asm volatile("" ::"v"(t[i]));
        }
        bar2_publish();
        wg_barrier();
        u32x4 ob[4];
#pragma unroll
        for (int t = 0; t < 4; ++t) ob[t] = *(const u32x4*)(lds + la + t * 64);
        __builtin_amdgcn_sched_barrier(0);
        f32x4 y0 = zero, y1 = zero;
#pragma unroll
        for (int t = 0; t < 4; ++t) { y0 = mfma(ob[t], af[2 * t], y0); y1 = mfma(ob[t], af[2 * t + 1], y1); }
        st_gran2(rws, yoff, y0[0], y1[0], tag);
        asm volatile(".Lfk_e2:");
    } else if (role == 1) {
      const int tp0 = 2 * wave + 4;
      const unsigned char* dl = lds + L_WT + 81920 + (8 * g + (c >> 2)) * DROW + tp0 * 64 + (c & 1) * 8 + (c & 2) * 16;
      const int zt = g * 40 + tp0 * 2 + (c >> 3), zl = ridx * 128 + e * 16;
      int zoff = zsl(zt) + zl, zoff2 = zsl(zt + 2) + zl, lg = L_GS + (c >> 2) * GROW + g * 16;
        u32x4 af[20], ad[4];
        const int row = 8 * ((c >> 2) & 1) + 16 * (g >> 1) + 4 * (g & 1) + (c >> 3);
        const unsigned char* wl = lds + L_WT + ((c & 3) >> 1) * 40960 + row * 64 + (wave - 4) * 16 + (c & 1) * 8;
        constexpr int TE = 20 - NLATE / 2;
#pragma unroll
        for (int t = 0; t < TE; ++t) af[t] = trfrag(wl + t * 2048, 128);
        __builtin_amdgcn_sched_barrier(0);
        const u32x4 ones = {0x3f803f80u, 0x3f803f80u, 0x3f803f80u, 0x3f803f80u};
        f32x4 cs4 = zero;
#pragma unroll
        for (int t = 0; t < TE; ++t) cs4 = mfma(ones, af[t], cs4);
        int lh = L_ACT + (c >> 2) * HROW + g * 16, lgw = L_GS + g * GROW + ((wave - 4) * 8 + e) * 2, ls = L_ST + g * 8;
        asm volatile("" : "+v"(cs4), "+v"(zoff), "+v"(zoff2), "+v"(lh), "+v"(lg), "+v"(lgw), "+v"(ls));
        bar2_publish();
        stage_wd();
#pragma unroll
        for (int t = TE; t < 20; ++t) af[t] = trfrag(wl + t * 2048, 128);
#pragma unroll
        for (int u = 0; u < 4; ++u) ad[u] = trfrag(dl + (u >> 1) * 64 + (u & 1) * 16, 4 * DROW);
#pragma unroll
        for (int t = TE; t < 20; ++t) cs4 = mfma(ones, af[t], cs4);
        float csum = cs4[0];
        asm volatile("" : "+v"(csum));
#pragma unroll
        for (int u = 0; u < 4; ++u) asm volatile("" : "+v"(ad[u]));
        wg_barrier();
        u32x4 hb[20];
        f32x4 a0 = zero, a1 = zero;
        f32x2 st;
#pragma unroll
        for (int t = 0; t < 8; ++t) hb[t] = *(const u32x4*)(lds + lh + t * 64);
        __builtin_amdgcn_sched_barrier(0);
        static_for<0, 20>([&](auto T) {
          constexpr int t = T.value;
          if constexpr (t + 8 < 20) hb[t + 8] = *(const u32x4*)(lds + lh + (t + 8) * 64);
          if constexpr (t == 15) { wg_barrier(); st = *(const f32x2*)(lds + ls); }
          if constexpr (t & 1) { a1 = mfma(hb[t], af[t], a1); asm volatile("" : "+v"(a1)); } else { a0 = mfma(hb[t], af[t], a0); asm volatile("" : "+v"(a0)); }
          __builtin_amdgcn_sched_barrier(0);
        });
        const float val = rbf(((a0[0] + a1[0]) - st[0] * csum) * st[1]);
        const float gv = rbf(gelu_tanh(val)) * dppf<0x128>(val);
        if (c < 8) *(u16*)(lds + lgw) = (u16)cvtpk(gv, gv);
        wg_barrier();
        const u32x4 gb = *(const u32x4*)(lds + lg);
        const f32x4 z0 = mfma(gb, ad[0], zero), z1 = mfma(gb, ad[1], zero), z2 = mfma(gb, ad[2], zero), z3 = mfma(gb, ad[3], zero);
        st_gran2(rws, zoff, z0[0], z1[0], tag);
        st_gran2(rws, zoff2, z2[0], z3[0], tag);
        asm volatile(".Lfk_e3:");
    } else {
      bar2_publish();
      if (role == 2) coordinate();
    }
  }
}

extern "C" void kernel_launch(void* const* d_in, const int* in_sizes, int n_in, void* d_out, int out_size, void* d_ws, size_t ws_size,
                              hipStream_t stream) {
  fused_kernel<<<256, 512, 0, stream>>>((const u16*)d_in[1], (const u16*)d_in[3], (const u16*)d_in[0], (unsigned char*)d_ws, (const u16*)d_in[2],
                                        (const u16*)d_in[4], (const u16*)d_in[6], (const u16*)d_in[7], (const u16*)d_in[8], (u16*)d_out);
}
```
